```python
import torch

def _softmax(x, axis=-1):
    return torch.softmax(x, dim=axis)
def _gelu(x):
    return torch.nn.functional.gelu(x, approximate="tanh")
def _silu(x):
    return torch.nn.functional.silu(x)
def _sigmoid(x):
    return torch.sigmoid(x)
def _layernorm(x, axis=-1, eps=1e-5):
    m = x.mean(dim=axis, keepdim=True)
    v = x.var(dim=axis, keepdim=True, unbiased=False)
    return (x - m) / torch.sqrt(v + eps)
def _scatter_add(tgt, idx, src, axis=0):
    return tgt.clone().index_add_(axis, idx, src)
def _segment_sum(src, seg, n):
    out = torch.zeros((n,) + src.shape[1:], dtype=src.dtype)
    return out.index_add_(0, seg, src)
def _pad(x, pw):
    flat = tuple(v for pair in reversed(pw) for v in pair)
    return torch.nn.functional.pad(x, flat)

kernel_name = 'standalone__glu_gated_geglu__decode'

def setup_inputs():

    _w = (lambda a: (lambda t: t.to(torch.bfloat16) if t.is_floating_point() else t)(torch.as_tensor(a)))
    B, S, Dm, Df = 1, 1, 1024, 3584
    return {
        "x": _w((lambda *s: torch.randn(*s, dtype=torch.bfloat16) * 0.05)(B, S, Dm)),
        "w_gate": _w((lambda *s: torch.randn(*s, dtype=torch.bfloat16) * 0.05)(Dm, Df) * (Dm ** -0.5)),
        "w_up": _w((lambda *s: torch.randn(*s, dtype=torch.bfloat16) * 0.05)(Dm, Df) * (Dm ** -0.5)),
        "w_down": _w((lambda *s: torch.randn(*s, dtype=torch.bfloat16) * 0.05)(Df, Dm) * (Df ** -0.5)),
    }

def reference(x, w_gate, w_up, w_down):
    g = torch.einsum("bsd,df->bsf", x, w_gate)
    u = torch.einsum("bsd,df->bsf", x, w_up)
    h = _gelu(g) * u
    return torch.einsum("bsf,fd->bsd", h, w_down)
```

```cpp
#include <hip/hip_runtime.h>
#include <hip/hip_bf16.h>
#include <stdint.h>

typedef unsigned v4u __attribute__((ext_vector_type(4)));
typedef unsigned v2u __attribute__((ext_vector_type(2)));
typedef float v4f __attribute__((ext_vector_type(4)));
typedef short v8s __attribute__((ext_vector_type(8)));
#define GAS __attribute__((address_space(1)))
typedef GAS unsigned gu32;
#define RLX_AGENT __ATOMIC_RELAXED, __HIP_MEMORY_SCOPE_AGENT

constexpr int DM = 1024, DF = 3584;
constexpr int NWG = 224;
constexpr int NXCD = 8, PER_XCD = NWG / NXCD;
constexpr int NRED = 128;
constexpr int MBOX_OFF = 4096;
constexpr int RED_BYTES = NWG * 64;
constexpr int MBOX_BYTES = NRED * RED_BYTES;
constexpr int AUX_NT = 2, AUX_SC1 = 16;
constexpr int RSRC_W3 = 0x00020000;

__device__ __forceinline__ float u2f(unsigned v) { return __builtin_bit_cast(float, v); }
__device__ __forceinline__ unsigned f2u(float v) { return __builtin_bit_cast(unsigned, v); }
__device__ __forceinline__ float bf_lo(unsigned v) { return __builtin_bit_cast(float, v << 16); }
__device__ __forceinline__ float bf_hi(unsigned v) { return __builtin_bit_cast(float, v & 0xffff0000u); }
__device__ __forceinline__ unsigned f2bf(float f) {
    __hip_bfloat16 b = __float2bfloat16(f);
    return (unsigned)__builtin_bit_cast(unsigned short, b);
}
__device__ __forceinline__ float rbf(float f) { return __builtin_bit_cast(float, f2bf(f) << 16); }

template <int CTRL> __device__ __forceinline__ float dpp_add(float v) {
    int t = __builtin_amdgcn_update_dpp(0, __builtin_bit_cast(int, v), CTRL, 0xf, 0xf, true);
    return v + __builtin_bit_cast(float, t);
}
__device__ __forceinline__ float swap32_add(float a, float b) {
    auto r = __builtin_amdgcn_permlane32_swap(__builtin_bit_cast(unsigned, a), __builtin_bit_cast(unsigned, b), false, false);
    return u2f(r[0]) + u2f(r[1]);
}
__device__ __forceinline__ float swap16_add(float a, float b) {
    auto r = __builtin_amdgcn_permlane16_swap(__builtin_bit_cast(unsigned, a), __builtin_bit_cast(unsigned, b), false, false);
    return u2f(r[0]) + u2f(r[1]);
}
constexpr int DPP_ROR(int n) { return 0x120 + n; }
constexpr int DPP_QUAD(int i) { return i | (i << 2) | (i << 4) | (i << 6); }

__global__ __launch_bounds__(512, 2) void geglu_decode_kernel(
    const unsigned short* __restrict__ x, const unsigned short* __restrict__ wg, const unsigned short* __restrict__ wu,
    const unsigned short* __restrict__ wd, unsigned short* __restrict__ out, unsigned char* ws)
{
    __shared__ __attribute__((aligned(16))) float lds[128 + 64];
    const int tid = threadIdx.x, l = tid & 63;
    const int w = __builtin_amdgcn_readfirstlane(tid >> 6);
    const int b = blockIdx.x;
    const int j = (b & (NXCD - 1)) * PER_XCD + (b >> 3);
    const int f0 = 16 * j;

    const int mat = w >> 2, ww = w & 3;
    const __amdgpu_buffer_rsrc_t rs_gu = __builtin_amdgcn_make_buffer_rsrc((void*)(mat ? wu : wg), 0, DM * DF * 2, RSRC_W3);
    const __amdgpu_buffer_rsrc_t rs_d = __builtin_amdgcn_make_buffer_rsrc((void*)wd, 0, DM * DF * 2, RSRC_W3);
    const __amdgpu_buffer_rsrc_t rs_x = __builtin_amdgcn_make_buffer_rsrc((void*)x, 0, DM * 2, RSRC_W3);
    const __amdgpu_buffer_rsrc_t rs_ws = __builtin_amdgcn_make_buffer_rsrc((void*)ws, 0, MBOX_OFF + MBOX_BYTES, RSRC_W3);

    const int row0 = 256 * ww + 8 * (l >> 1);
    const v4u xv = __builtin_amdgcn_raw_buffer_load_b128(rs_x, row0 * 2, 0, 0);
    const int voff = row0 * (DF * 2) + (f0 + 8 * (l & 1)) * 2;
    v4u wv[8];
#pragma unroll
    for (int i = 0; i < 8; ++i) wv[i] = __builtin_amdgcn_raw_buffer_load_b128(rs_gu, voff, i * (DF * 2), AUX_NT);
    const int q = l >> 4, n = l & 15;
    const int doff = (f0 + 4 * q) * (DM * 2) + (128 * w + 8 * n) * 2;
    v4u dv[4];
#pragma unroll
    for (int i = 0; i < 4; ++i) dv[i] = __builtin_amdgcn_raw_buffer_load_b128(rs_d, doff, i * (DM * 2), AUX_NT);
    const unsigned ep = __builtin_amdgcn_raw_buffer_load_b32(rs_ws, 0, 0, AUX_SC1);
    __builtin_amdgcn_sched_barrier(0);

    float acc[8];
#pragma unroll
    for (int k = 0; k < 8; ++k) acc[k] = 0.f;
#pragma unroll
    for (int i = 0; i < 8; ++i) {
        const unsigned xr = xv[i >> 1];
        const float xi = (i & 1) ? bf_hi(xr) : bf_lo(xr);
#pragma unroll
        for (int k = 0; k < 4; ++k) {
            acc[2 * k] = fmaf(xi, bf_lo(wv[i][k]), acc[2 * k]);
            acc[2 * k + 1] = fmaf(xi, bf_hi(wv[i][k]), acc[2 * k + 1]);
        }
    }
    float b0 = swap32_add(acc[0], acc[4]), b1 = swap32_add(acc[1], acc[5]);
    float b2 = swap32_add(acc[2], acc[6]), b3 = swap32_add(acc[3], acc[7]);
    float c0 = swap16_add(b0, b2), c1 = swap16_add(b1, b3);
    c0 = dpp_add<DPP_ROR(8)>(c0); c1 = dpp_add<DPP_ROR(8)>(c1);
    c0 = dpp_add<DPP_ROR(4)>(c0); c1 = dpp_add<DPP_ROR(4)>(c1);
    c0 = dpp_add<DPP_ROR(2)>(c0); c1 = dpp_add<DPP_ROR(2)>(c1);
    if ((l & 15) < 2) {
        const int col = 8 * (l & 1) + 2 * q;
        lds[(mat * 16 + col) * 4 + ww] = c0;
        lds[(mat * 16 + col + 1) * 4 + ww] = c1;
    }
    __syncthreads();

    const int c = 4 * q + (l & 3);
    const v4f g4 = *(const v4f*)&lds[c * 4], u4 = *(const v4f*)&lds[(16 + c) * 4];
    const float g = rbf((g4[0] + g4[1]) + (g4[2] + g4[3]));
    const float u = rbf((u4[0] + u4[1]) + (u4[2] + u4[3]));
    const float y2 = 1.5957691216057308f * (g + 0.044715f * g * g * g);
    const float ge = rbf(g / (1.0f + __expf(-y2)));
    const unsigned hb = f2bf(ge * u);
    const unsigned hpk = hb | (hb << 16);
    const int m = l & 15;
    const unsigned msk = ((m & 3) < 2) ? ((m & 1) ? 0xffff0000u : 0x0000ffffu) : 0u;
    unsigned mk[4];
#pragma unroll
    for (int r = 0; r < 4; ++r) mk[r] = ((m >> 2) == r) ? msk : 0u;

    v4f o = {0.f, 0.f, 0.f, 0.f};
#define STEP(i) { const unsigned hh = (unsigned)__builtin_amdgcn_update_dpp(0, (int)hpk, DPP_QUAD(i), 0xf, 0xf, true); \
        v4u a = {hh & mk[0], hh & mk[1], hh & mk[2], hh & mk[3]}; \
        o = __builtin_amdgcn_mfma_f32_16x16x32_bf16(__builtin_bit_cast(v8s, a), __builtin_bit_cast(v8s, dv[i]), o, 0, 0, 0); }
    STEP(0) STEP(1) STEP(2) STEP(3)
#undef STEP

    const unsigned tag = ep * 1664525u + 1013904223u;
    {
        v4u p = {f2u(o[0]), tag, f2u(o[1]), tag};
        const int off = MBOX_OFF + (16 * w + n) * RED_BYTES + j * 64 + q * 16;
        __builtin_amdgcn_raw_buffer_store_b128(p, rs_ws, off, 0, AUX_SC1);
    }

    if (b >= NRED) return;
    float s0 = 0.f, s1 = 0.f;
    if (w < 7) {
        const int off = MBOX_OFF + b * RED_BYTES + tid * 16;
        v4u p0, p1;
        for (unsigned spins = 0; spins < (1u << 22); ++spins) {
            asm volatile("" ::: "memory");
            p0 = __builtin_amdgcn_raw_buffer_load_b128(rs_ws, off, 0, AUX_SC1);
            p1 = __builtin_amdgcn_raw_buffer_load_b128(rs_ws, off, 448 * 16, AUX_SC1);
            const bool ok = (p0[1] == tag) & (p0[3] == tag) & (p1[1] == tag) & (p1[3] == tag);
            if (__all(ok)) break;
            __builtin_amdgcn_s_sleep(1);
        }
        s0 = u2f(p0[0]) + u2f(p1[0]);
        s1 = u2f(p0[2]) + u2f(p1[2]);
        float s = swap32_add(s0, s1);
        s = swap16_add(s, s);
        s = dpp_add<DPP_ROR(4)>(s);
        s = dpp_add<DPP_ROR(8)>(s);
        if ((l & 31) < 4) lds[128 + (2 * (l & 3) + (l >> 5)) * 8 + w] = s;
    }
    __syncthreads();
    if (tid < 8) {
        const v4f a = *(const v4f*)&lds[128 + tid * 8], c4 = *(const v4f*)&lds[128 + tid * 8 + 4];
        const float s = ((a[0] + a[1]) + (a[2] + a[3])) + ((c4[0] + c4[1]) + c4[2]);
        out[8 * b + tid] = (unsigned short)f2bf(s);
        if (b == 0 && tid == 0) __hip_atomic_store((gu32*)ws, tag, RLX_AGENT);
    }
}

extern "C" void kernel_launch(void* const* d_in, const int* in_sizes, int n_in,
                              void* d_out, int out_size, void* d_ws, size_t ws_size,
                              hipStream_t stream) {
    geglu_decode_kernel<<<NWG, 512, 0, stream>>>((const unsigned short*)d_in[0], (const unsigned short*)d_in[1],
        (const unsigned short*)d_in[2], (const unsigned short*)d_in[3], (unsigned short*)d_out, (unsigned char*)d_ws);
}
```

```cpp
#include <hip/hip_runtime.h>
#include <hip/hip_bf16.h>
#include <stdint.h>

typedef unsigned v4u __attribute__((ext_vector_type(4)));
typedef float v4f __attribute__((ext_vector_type(4)));
typedef short v8s __attribute__((ext_vector_type(8)));
#define GAS __attribute__((address_space(1)))
typedef GAS unsigned gu32;
#define RLX_AGENT __ATOMIC_RELAXED, __HIP_MEMORY_SCOPE_AGENT

constexpr int DM = 1024, DF = 3584;
constexpr int NWG = 224;
constexpr int NXCD = 8, PER_XCD = NWG / NXCD;
constexpr int NRED = 128;
constexpr int MB1_OFF = 4096;
constexpr int MB1_BYTES = NWG * 1024;
constexpr int MB2_OFF = MB1_OFF + MB1_BYTES;
constexpr int RED_BYTES = NWG * 64;
constexpr int MB2_BYTES = NRED * RED_BYTES;
constexpr int WS_BYTES = MB2_OFF + MB2_BYTES;
constexpr int AUX_NT = 2, AUX_SC1 = 16;
constexpr int RSRC_W3 = 0x00020000;

__device__ __forceinline__ float u2f(unsigned v) { return __builtin_bit_cast(float, v); }
__device__ __forceinline__ unsigned f2u(float v) { return __builtin_bit_cast(unsigned, v); }
__device__ __forceinline__ float bf_lo(unsigned v) { return __builtin_bit_cast(float, v << 16); }
__device__ __forceinline__ float bf_hi(unsigned v) { return __builtin_bit_cast(float, v & 0xffff0000u); }
__device__ __forceinline__ unsigned f2bf(float f) {
    __hip_bfloat16 b = __float2bfloat16(f);
    return (unsigned)__builtin_bit_cast(unsigned short, b);
}
__device__ __forceinline__ float rbf(float f) { return __builtin_bit_cast(float, f2bf(f) << 16); }

template <int CTRL> __device__ __forceinline__ float dpp_add(float v) {
    int t = __builtin_amdgcn_update_dpp(0, __builtin_bit_cast(int, v), CTRL, 0xf, 0xf, true);
    return v + __builtin_bit_cast(float, t);
}
__device__ __forceinline__ float swap32_add(float a, float b) {
    auto r = __builtin_amdgcn_permlane32_swap(f2u(a), f2u(b), false, false);
    return u2f(r[0]) + u2f(r[1]);
}
__device__ __forceinline__ float swap16_add(float a, float b) {
    auto r = __builtin_amdgcn_permlane16_swap(f2u(a), f2u(b), false, false);
    return u2f(r[0]) + u2f(r[1]);
}
#define STAMP(k)
#define STAMP_DECL
#define STAMP_FLUSH()
constexpr int DPP_ROR(int n) { return 0x120 + n; }
constexpr int DPP_QUAD(int i) { return i | (i << 2) | (i << 4) | (i << 6); }
constexpr unsigned SPIN_CAP = 1u << 22;

__global__ __launch_bounds__(512, 2) void geglu_decode_kernel(
    const unsigned short* __restrict__ x, const unsigned short* __restrict__ wg, const unsigned short* __restrict__ wu,
    const unsigned short* __restrict__ wd, unsigned short* out, unsigned char* ws)
{
    STAMP_DECL
    STAMP(0);
    __shared__ __attribute__((aligned(16))) float lds[512 + 64];
    const int tid = threadIdx.x, l = tid & 63;
    const int w = __builtin_amdgcn_readfirstlane(tid >> 6);
    const int b = blockIdx.x;
    const int j = (b & (NXCD - 1)) * PER_XCD + (b >> 3);
    const int cb = j >> 2, kp = j & 3;

    const int mat = w >> 2, ww = w & 3;
    const __amdgpu_buffer_rsrc_t rs_gu = __builtin_amdgcn_make_buffer_rsrc((void*)(mat ? wu : wg), 0, DM * DF * 2, RSRC_W3);
    const __amdgpu_buffer_rsrc_t rs_d = __builtin_amdgcn_make_buffer_rsrc((void*)wd, 0, DM * DF * 2, RSRC_W3);
    const __amdgpu_buffer_rsrc_t rs_x = __builtin_amdgcn_make_buffer_rsrc((void*)x, 0, DM * 2, RSRC_W3);
    const __amdgpu_buffer_rsrc_t rs_ws = __builtin_amdgcn_make_buffer_rsrc((void*)ws, 0, WS_BYTES, RSRC_W3);
    const __amdgpu_buffer_rsrc_t rs_o = __builtin_amdgcn_make_buffer_rsrc((void*)out, 0, DM * 2, RSRC_W3);

    const int row0 = 256 * kp + 64 * ww + 8 * (l >> 3);
    const int voff = row0 * (DF * 2) + (64 * cb + 8 * (l & 7)) * 2;
    v4u wv[8];
    wv[0] = __builtin_amdgcn_raw_buffer_load_b128(rs_gu, voff, 0, AUX_NT);
    const v4u xv = __builtin_amdgcn_raw_buffer_load_b128(rs_x, row0 * 2, 0, 0);
    const unsigned ep = __builtin_amdgcn_raw_buffer_load_b32(rs_ws, 0, 0, AUX_SC1);
#pragma unroll
    for (int i = 1; i < 8; ++i) wv[i] = __builtin_amdgcn_raw_buffer_load_b128(rs_gu, voff, i * (DF * 2), AUX_NT);
    const int q = l >> 4, n = l & 15;
    const int doff = (16 * j + 4 * q) * (DM * 2) + (128 * w + 8 * n) * 2;
    v4u dv[4];
#pragma unroll
    for (int i = 0; i < 4; ++i) dv[i] = __builtin_amdgcn_raw_buffer_load_b128(rs_d, doff, i * (DM * 2), AUX_NT);
    __builtin_amdgcn_sched_barrier(0);
    STAMP(2);

    float acc[8];
#pragma unroll
    for (int k = 0; k < 8; ++k) acc[k] = 0.f;
#pragma unroll
    for (int i = 0; i < 8; ++i) {
        const unsigned xr = xv[i >> 1];
        const float xi = (i & 1) ? bf_hi(xr) : bf_lo(xr);
#pragma unroll
        for (int k = 0; k < 4; ++k) {
            acc[2 * k] = fmaf(xi, bf_lo(wv[i][k]), acc[2 * k]);
            acc[2 * k + 1] = fmaf(xi, bf_hi(wv[i][k]), acc[2 * k + 1]);
        }
    }
    STAMP(3);
    {
        float b0 = swap32_add(acc[0], acc[4]), b1 = swap32_add(acc[1], acc[5]);
        float b2 = swap32_add(acc[2], acc[6]), b3 = swap32_add(acc[3], acc[7]);
        float c0 = swap16_add(b0, b2), c1 = swap16_add(b1, b3);
        c0 = dpp_add<DPP_ROR(8)>(c0); c1 = dpp_add<DPP_ROR(8)>(c1);
        if ((l & 8) == 0) {
            const int col = 8 * (l & 7) + 2 * q;
            lds[(mat * 64 + col) * 4 + ww] = c0;
            lds[(mat * 64 + col + 1) * 4 + ww] = c1;
        }
    }
    __syncthreads();
    STAMP(4);
    const unsigned tag = ep * 1664525u + 1013904223u;
    if (w == 0) {
        const v4f g4 = *(const v4f*)&lds[l * 4], u4 = *(const v4f*)&lds[(64 + l) * 4];
        const float g = (g4[0] + g4[1]) + (g4[2] + g4[3]), u = (u4[0] + u4[1]) + (u4[2] + u4[3]);
        const v4u p = {f2u(g), tag, f2u(u), tag};
        const int c = l & 15;
        const int off = MB1_OFF + (4 * cb + (l >> 4)) * 1024 + ((c >> 2) * 16 + kp * 4 + (c & 3)) * 16;
        __builtin_amdgcn_raw_buffer_store_b128(p, rs_ws, off, 0, AUX_SC1);
    }
    STAMP(5);

    float g, u;
    {
        const int off = MB1_OFF + j * 1024 + l * 16;
        v4u p;
        for (unsigned spins = 0; spins < SPIN_CAP; ++spins) {
            asm volatile("" ::: "memory");
            p = __builtin_amdgcn_raw_buffer_load_b128(rs_ws, off, 0, AUX_SC1);
            const bool ok = (p[1] == tag) & (p[3] == tag);
            if (__all(ok)) break;
        }
        g = u2f(p[0]); u = u2f(p[2]);
        g = dpp_add<DPP_ROR(4)>(g); u = dpp_add<DPP_ROR(4)>(u);
        g = dpp_add<DPP_ROR(8)>(g); u = dpp_add<DPP_ROR(8)>(u);
    }
    STAMP(6);
    g = rbf(g); u = rbf(u);
    const float y2 = 1.5957691216057308f * (g + 0.044715f * g * g * g);
    const float ge = rbf(g / (1.0f + __expf(-y2)));
    const unsigned hb = f2bf(ge * u);
    const unsigned hpk = hb | (hb << 16);
    const int m = l & 15;
    const unsigned msk = ((m & 3) < 2) ? ((m & 1) ? 0xffff0000u : 0x0000ffffu) : 0u;
    unsigned mk[4];
#pragma unroll
    for (int r = 0; r < 4; ++r) mk[r] = ((m >> 2) == r) ? msk : 0u;

    v4f o = {0.f, 0.f, 0.f, 0.f};
#define STEP(i) { const unsigned hh = (unsigned)__builtin_amdgcn_update_dpp(0, (int)hpk, DPP_QUAD(i), 0xf, 0xf, true); \
        v4u a = {hh & mk[0], hh & mk[1], hh & mk[2], hh & mk[3]}; \
        o = __builtin_amdgcn_mfma_f32_16x16x32_bf16(__builtin_bit_cast(v8s, a), __builtin_bit_cast(v8s, dv[i]), o, 0, 0, 0); }
    STEP(0) STEP(1) STEP(2) STEP(3)
#undef STEP

    {
        v4u p = {f2u(o[0]), tag, f2u(o[1]), tag};
        const int off = MB2_OFF + (16 * w + n) * RED_BYTES + j * 64 + q * 16;
        __builtin_amdgcn_raw_buffer_store_b128(p, rs_ws, off, 0, AUX_SC1);
    }
    STAMP(7);

    if (b >= NRED) { STAMP_FLUSH(); return; }
    if (w < 7) {
        const int off = MB2_OFF + b * RED_BYTES + tid * 16;
        v4u p0, p1;
        for (unsigned spins = 0; spins < SPIN_CAP; ++spins) {
            asm volatile("" ::: "memory");
            p0 = __builtin_amdgcn_raw_buffer_load_b128(rs_ws, off, 0, AUX_SC1);
            p1 = __builtin_amdgcn_raw_buffer_load_b128(rs_ws, off, 448 * 16, AUX_SC1);
            const bool ok = (p0[1] == tag) & (p0[3] == tag) & (p1[1] == tag) & (p1[3] == tag);
            if (__all(ok)) break;
        }
        STAMP(8);
        const float s0 = u2f(p0[0]) + u2f(p1[0]);
        const float s1 = u2f(p0[2]) + u2f(p1[2]);
        float s = swap32_add(s0, s1);
        s = swap16_add(s, s);
        s = dpp_add<DPP_ROR(4)>(s);
        s = dpp_add<DPP_ROR(8)>(s);
        if ((l & 31) < 4) lds[512 + (2 * (l & 3) + (l >> 5)) * 8 + w] = s;
    }
    __syncthreads();
    STAMP(9);
    if (tid < 8) {
        const v4f a = *(const v4f*)&lds[512 + tid * 8], c4 = *(const v4f*)&lds[512 + tid * 8 + 4];
        const float s = ((a[0] + a[1]) + (a[2] + a[3])) + ((c4[0] + c4[1]) + c4[2]);
        __builtin_amdgcn_raw_buffer_store_b16((unsigned short)f2bf(s), rs_o, (8 * b + tid) * 2, 0, 0);
        if (b == 0 && tid == 0) __hip_atomic_store((gu32*)ws, tag, RLX_AGENT);
    }
    STAMP(10);
    STAMP_FLUSH();
}

extern "C" void kernel_launch(void* const* d_in, const int* in_sizes, int n_in,
                              void* d_out, int out_size, void* d_ws, size_t ws_size,
                              hipStream_t stream) {
    geglu_decode_kernel<<<NWG, 512, 0, stream>>>((const unsigned short*)d_in[0], (const unsigned short*)d_in[1],
        (const unsigned short*)d_in[2], (const unsigned short*)d_in[3], (unsigned short*)d_out, (unsigned char*)d_ws);
}
```

```cpp
#include <hip/hip_runtime.h>
#include <hip/hip_bf16.h>
#include <stdint.h>

typedef unsigned v4u __attribute__((ext_vector_type(4)));
typedef float v4f __attribute__((ext_vector_type(4)));
typedef float v2f __attribute__((ext_vector_type(2)));
typedef short v8s __attribute__((ext_vector_type(8)));
#define GAS __attribute__((address_space(1)))
typedef GAS unsigned gu32;
#define RLX_AGENT __ATOMIC_RELAXED, __HIP_MEMORY_SCOPE_AGENT

constexpr int DM = 1024, DF = 3584;
constexpr int NWG = 224;
constexpr int NGRP = 28;
constexpr int NXCD = 8, PER_XCD = NWG / NXCD;
constexpr int NRED = 64;
constexpr int MB1_OFF = 4096;
constexpr int MB1_BYTES = NWG * 2048;
constexpr int MB2_OFF = MB1_OFF + MB1_BYTES;
constexpr int RED_BYTES = 4096;
constexpr int MB2_BYTES = NRED * RED_BYTES;
constexpr int WS_BYTES = MB2_OFF + MB2_BYTES;
constexpr int AUX_NT = 2, AUX_SC1 = 16;
constexpr int RSRC_W3 = 0x00020000;

__device__ __forceinline__ float u2f(unsigned v) { return __builtin_bit_cast(float, v); }
__device__ __forceinline__ unsigned f2u(float v) { return __builtin_bit_cast(unsigned, v); }
__device__ __forceinline__ float bf_lo(unsigned v) { return __builtin_bit_cast(float, v << 16); }
__device__ __forceinline__ float bf_hi(unsigned v) { return __builtin_bit_cast(float, v & 0xffff0000u); }
__device__ __forceinline__ unsigned f2bf(float f) {
    __hip_bfloat16 b = __float2bfloat16(f);
    return (unsigned)__builtin_bit_cast(unsigned short, b);
}
__device__ __forceinline__ float rbf(float f) { return __builtin_bit_cast(float, f2bf(f) << 16); }

template <int CTRL> __device__ __forceinline__ float dpp_add(float v) {
    int t = __builtin_amdgcn_update_dpp(0, __builtin_bit_cast(int, v), CTRL, 0xf, 0xf, true);
    return v + __builtin_bit_cast(float, t);
}
__device__ __forceinline__ float swap32_add(float a, float b) {
    auto r = __builtin_amdgcn_permlane32_swap(f2u(a), f2u(b), false, false);
    return u2f(r[0]) + u2f(r[1]);
}
__device__ __forceinline__ float swap16_add(float a, float b) {
    auto r = __builtin_amdgcn_permlane16_swap(f2u(a), f2u(b), false, false);
    return u2f(r[0]) + u2f(r[1]);
}
#define STAMP(k)
#define STAMP_DECL
#define STAMP_FLUSH()
constexpr int DPP_ROR(int n) { return 0x120 + n; }
constexpr int DPP_QUAD(int i) { return i | (i << 2) | (i << 4) | (i << 6); }
#define PREWARM 0
#define WD_DELAY 8

#define POLL_ISSUE(R0, R1) \
    "buffer_load_dwordx4 " R0 ", %[voff], %[rs], 0 offen sc1\n\t" \
    "buffer_load_dwordx4 " R1 ", %[voff2], %[rs], 0 offen sc1\n\t"
#define POLL_CHECK(X0, Y0, Z0, W0, X1, Y1, Z1, W1) \
    "s_waitcnt vmcnt(4)\n\t" \
    "v_cmp_ne_u32_e32 vcc, %[tag], " Y0 "\n\t" \
    "v_cmp_ne_u32_e64 %[t0], %[tag], " W0 "\n\t" \
    "v_cmp_ne_u32_e64 %[t1], %[tag], " Y1 "\n\t" \
    "v_cmp_ne_u32_e64 %[t2], %[tag], " W1 "\n\t" \
    "s_or_b64 vcc, vcc, %[t0]\n\t" \
    "s_or_b64 %[t1], %[t1], %[t2]\n\t" \
    "s_or_b64 %[t0], vcc, %[t1]\n\t" \
    "s_andn2_b64 exec, exec, %[t0]\n\t" \
    "v_mov_b32 %[r0], " X0 "\n\t" \
    "v_mov_b32 %[r1], " Z0 "\n\t" \
    "v_mov_b32 %[r2], " X1 "\n\t" \
    "v_mov_b32 %[r3], " Z1 "\n\t" \
    "s_mov_b64 exec, %[t0]\n\t" \
    "s_cmp_eq_u64 %[t0], 0\n\t" \
    "s_cbranch_scc1 2f\n\t"
#define POLL_BODY(SKIP) \
    asm volatile( \
        "s_cmp_eq_u32 %[sel], 0\n\t" \
        SKIP " 3f\n\t" \
        "s_mov_b64 %[save], exec\n\t" \
        "s_mov_b32 %[cnt], 0x100000\n\t" \
        "s_nop 3\n\t" \
        POLL_ISSUE("v[104:107]", "v[108:111]") \
        POLL_ISSUE("v[112:115]", "v[116:119]") \
        "1:\n\t" \
        POLL_ISSUE("v[120:123]", "v[124:127]") \
        POLL_CHECK("v104", "v105", "v106", "v107", "v108", "v109", "v110", "v111") \
        POLL_ISSUE("v[104:107]", "v[108:111]") \
        POLL_CHECK("v112", "v113", "v114", "v115", "v116", "v117", "v118", "v119") \
        POLL_ISSUE("v[112:115]", "v[116:119]") \
        POLL_CHECK("v120", "v121", "v122", "v123", "v124", "v125", "v126", "v127") \
        "s_sub_u32 %[cnt], %[cnt], 1\n\t" \
        "s_cbranch_scc0 1b\n\t" \
        "2:\n\t" \
        "s_mov_b64 exec, %[save]\n\t" \
        "3:\n\t" \
        : [r0] "=&v"(r0), [r1] "=&v"(r1), [r2] "=&v"(r2), [r3] "=&v"(r3), [save] "=&s"(save), [t0] "=&s"(t0), [t1] "=&s"(t1), [t2] "=&s"(t2), [cnt] "=&s"(cnt) \
        : [voff] "v"(voff), [voff2] "v"(voff2), [rs] "s"(rs), [tag] "s"(tag), [sel] "s"(sel) \
        : "memory", "vcc", "scc", \
          "v104", "v105", "v106", "v107", "v108", "v109", "v110", "v111", "v112", "v113", "v114", "v115", \
          "v116", "v117", "v118", "v119", "v120", "v121", "v122", "v123", "v124", "v125", "v126", "v127")
#define POLL1_BODY(SLEEP) \
    asm volatile( \
        "s_mov_b64 %[save], exec\n\t" \
        "s_mov_b32 %[cnt], 0x100000\n\t" \
        "s_nop 3\n\t" \
        "1:\n\t" \
        POLL_ISSUE("v[104:107]", "v[108:111]") \
        "s_waitcnt vmcnt(0)\n\t" \
        "v_cmp_ne_u32_e32 vcc, %[tag], v105\n\t" \
        "v_cmp_ne_u32_e64 %[t0], %[tag], v107\n\t" \
        "v_cmp_ne_u32_e64 %[t1], %[tag], v109\n\t" \
        "v_cmp_ne_u32_e64 %[t2], %[tag], v111\n\t" \
        "s_or_b64 vcc, vcc, %[t0]\n\t" \
        "s_or_b64 %[t1], %[t1], %[t2]\n\t" \
        "s_or_b64 exec, vcc, %[t1]\n\t" \
        "s_cbranch_execz 2f\n\t" \
        SLEEP \
        "s_sub_u32 %[cnt], %[cnt], 1\n\t" \
        "s_cbranch_scc0 1b\n\t" \
        "2:\n\t" \
        "s_mov_b64 exec, %[save]\n\t" \
        "v_mov_b32 %[r0], v104\n\t" \
        "v_mov_b32 %[r1], v106\n\t" \
        "v_mov_b32 %[r2], v108\n\t" \
        "v_mov_b32 %[r3], v110\n\t" \
        : [r0] "=&v"(r0), [r1] "=&v"(r1), [r2] "=&v"(r2), [r3] "=&v"(r3), [save] "=&s"(save), [t0] "=&s"(t0), [t1] "=&s"(t1), [t2] "=&s"(t2), [cnt] "=&s"(cnt) \
        : [voff] "v"(voff), [voff2] "v"(voff2), [rs] "s"(rs), [tag] "s"(tag) \
        : "memory", "vcc", "scc", "v104", "v105", "v106", "v107", "v108", "v109", "v110", "v111")
template <int SLP>
__device__ __forceinline__ void poll1(__amdgpu_buffer_rsrc_t rs, int voff, int voff2, unsigned tag, float& r0, float& r1, float& r2, float& r3)
{
    unsigned long long save, t0, t1, t2; unsigned cnt;
    if constexpr (SLP == 0) { POLL1_BODY(""); } else if constexpr (SLP == 1) { POLL1_BODY("s_sleep 2\n\t"); } else { POLL1_BODY("s_sleep 6\n\t"); }
}
__device__ __forceinline__ void poll4(__amdgpu_buffer_rsrc_t rs, int voff, int voff4, unsigned tag, float (&r)[8])
{
    unsigned long long save, t0, t1, t2; unsigned cnt;
    asm volatile(
        "s_mov_b64 %[save], exec\n\t"
        "s_mov_b32 %[cnt], 0x100000\n\t"
        "s_nop 3\n\t"
        "1:\n\t"
        "buffer_load_dwordx4 v[104:107], %[voff], %[rs], 0 offen sc1\n\t"
        "buffer_load_dwordx4 v[108:111], %[voff], %[rs], 0 offen offset:1024 sc1\n\t"
        "buffer_load_dwordx4 v[112:115], %[voff], %[rs], 0 offen offset:2048 sc1\n\t"
        "buffer_load_dwordx4 v[116:119], %[voff4], %[rs], 0 offen sc1\n\t"
        "s_waitcnt vmcnt(2)\n\t"
        "v_cmp_ne_u32_e32 vcc, %[tag], v105\n\t"
        "v_cmp_ne_u32_e64 %[t0], %[tag], v107\n\t"
        "v_cmp_ne_u32_e64 %[t1], %[tag], v109\n\t"
        "v_cmp_ne_u32_e64 %[t2], %[tag], v111\n\t"
        "s_or_b64 vcc, vcc, %[t0]\n\t"
        "s_or_b64 %[t1], %[t1], %[t2]\n\t"
        "s_or_b64 %[t0], vcc, %[t1]\n\t"
        "s_waitcnt vmcnt(0)\n\t"
        "v_cmp_ne_u32_e32 vcc, %[tag], v113\n\t"
        "v_cmp_ne_u32_e64 %[t1], %[tag], v115\n\t"
        "s_or_b64 %[t0], %[t0], vcc\n\t"
        "v_cmp_ne_u32_e32 vcc, %[tag], v117\n\t"
        "v_cmp_ne_u32_e64 %[t2], %[tag], v119\n\t"
        "s_or_b64 %[t0], %[t0], %[t1]\n\t"
        "s_or_b64 vcc, vcc, %[t2]\n\t"
        "s_or_b64 exec, vcc, %[t0]\n\t"
        "s_cbranch_execz 2f\n\t"
        "s_sub_u32 %[cnt], %[cnt], 1\n\t"
        "s_cbranch_scc0 1b\n\t"
        "2:\n\t"
        "s_mov_b64 exec, %[save]\n\t"
        "v_mov_b32 %[r0], v104\n\t"
        "v_mov_b32 %[r1], v106\n\t"
        "v_mov_b32 %[r2], v108\n\t"
        "v_mov_b32 %[r3], v110\n\t"
        "v_mov_b32 %[r4], v112\n\t"
        "v_mov_b32 %[r5], v114\n\t"
        "v_mov_b32 %[r6], v116\n\t"
        "v_mov_b32 %[r7], v118\n\t"
        : [r0] "=&v"(r[0]), [r1] "=&v"(r[1]), [r2] "=&v"(r[2]), [r3] "=&v"(r[3]), [r4] "=&v"(r[4]), [r5] "=&v"(r[5]), [r6] "=&v"(r[6]), [r7] "=&v"(r[7]),
          [save] "=&s"(save), [t0] "=&s"(t0), [t1] "=&s"(t1), [t2] "=&s"(t2), [cnt] "=&s"(cnt)
        : [voff] "v"(voff), [voff4] "v"(voff4), [rs] "s"(rs), [tag] "s"(tag)
        : "memory", "vcc", "scc", "v104", "v105", "v106", "v107", "v108", "v109", "v110", "v111", "v112", "v113", "v114", "v115", "v116", "v117", "v118", "v119");
}
__device__ __forceinline__ void poll2(__amdgpu_buffer_rsrc_t rs, int voff, int voff2, unsigned tag, int sel, float& r0, float& r1, float& r2, float& r3)
{
    unsigned long long save, t0, t1, t2; unsigned cnt;
    POLL_BODY("s_cbranch_scc0");
}

__global__ __launch_bounds__(512, 2) void geglu_decode_kernel(
    const unsigned short* __restrict__ x, const unsigned short* __restrict__ wg, const unsigned short* __restrict__ wu,
    const unsigned short* __restrict__ wd, unsigned short* out, unsigned char* ws)
{
    STAMP_DECL
    STAMP(0);
    __shared__ __attribute__((aligned(16))) float lds[2048];
    const int tid = threadIdx.x, l = tid & 63;
    const int w = __builtin_amdgcn_readfirstlane(tid >> 6);
    const int b = blockIdx.x;
    const int j = (b & (NXCD - 1)) * PER_XCD + (b >> 3);
    const int cb = j >> 3, kp = j & 7;

    const int mat = w >> 2, ww = w & 3;
    const __amdgpu_buffer_rsrc_t rs_gu = __builtin_amdgcn_make_buffer_rsrc((void*)(mat ? wu : wg), 0, DM * DF * 2, RSRC_W3);
    const __amdgpu_buffer_rsrc_t rs_d = __builtin_amdgcn_make_buffer_rsrc((void*)wd, 0, DM * DF * 2, RSRC_W3);
    const __amdgpu_buffer_rsrc_t rs_x = __builtin_amdgcn_make_buffer_rsrc((void*)x, 0, DM * 2, RSRC_W3);
    const __amdgpu_buffer_rsrc_t rs_ws = __builtin_amdgcn_make_buffer_rsrc((void*)ws, 0, WS_BYTES, RSRC_W3);
    const __amdgpu_buffer_rsrc_t rs_o = __builtin_amdgcn_make_buffer_rsrc((void*)out, 0, DM * 2, RSRC_W3);

    unsigned ep;
    asm volatile("s_load_dword %0, %1, 0x0" : "=s"(ep) : "s"(ws), "s"(x), "s"(wg), "s"(wu), "s"(wd), "s"(out) : "memory");
    const int q = l >> 4, n = l & 15;
    const int row0 = 128 * kp + 32 * ww + 8 * q;
    const int voff = row0 * (DF * 2) + (128 * cb + 8 * n) * 2;
    v4u wv[8];
    wv[0] = __builtin_amdgcn_raw_buffer_load_b128(rs_gu, voff, 0, AUX_NT);
    const v4u xv = __builtin_amdgcn_raw_buffer_load_b128(rs_x, row0 * 2, 0, 0);
#pragma unroll
    for (int i = 1; i < 8; ++i) wv[i] = __builtin_amdgcn_raw_buffer_load_b128(rs_gu, voff, i * (DF * 2), AUX_NT);
    const int doff = (128 * cb + 16 * w + 4 * q) * (DM * 2) + (128 * kp + 8 * n) * 2;
    v4u dv[4];
    __builtin_amdgcn_sched_barrier(0);
    STAMP(1);

    float acc[8];
#pragma unroll
    for (int k = 0; k < 8; ++k) acc[k] = 0.f;
#pragma unroll
    for (int i = 0; i <= 8; ++i) {
        if (i == WD_DELAY) {
            if (i > 0) asm volatile("" :: "v"(wv[i - 1]));
            __builtin_amdgcn_sched_barrier(0);
#pragma unroll
            for (int k = 0; k < 4; ++k) dv[k] = __builtin_amdgcn_raw_buffer_load_b128(rs_d, doff, k * (DM * 2), AUX_NT);
            __builtin_amdgcn_sched_barrier(0);
        }
        if (i < 8) {
            const unsigned xr = xv[i >> 1];
            const float xi = (i & 1) ? bf_hi(xr) : bf_lo(xr);
#pragma unroll
            for (int k = 0; k < 4; ++k) {
                acc[2 * k] = fmaf(xi, bf_lo(wv[i][k]), acc[2 * k]);
                acc[2 * k + 1] = fmaf(xi, bf_hi(wv[i][k]), acc[2 * k + 1]);
            }
        }
    }
    STAMP(2);
    asm volatile("s_waitcnt lgkmcnt(0)" : "+s"(ep) :: "memory");
    {
        const float b0 = swap32_add(acc[0], acc[4]), b1 = swap32_add(acc[1], acc[5]);
        const float b2 = swap32_add(acc[2], acc[6]), b3 = swap32_add(acc[3], acc[7]);
        v2f c; c[0] = swap16_add(b0, b2); c[1] = swap16_add(b1, b3);
        *(v2f*)&lds[w * 128 + 8 * n + 2 * q] = c;
    }
    const unsigned tag = ep * 1664525u + 1013904223u;
    const unsigned msk = ((n & 3) < 2) ? ((n & 1) ? 0xffff0000u : 0x0000ffffu) : 0u;
    unsigned mk[4];
#pragma unroll
    for (int r = 0; r < 4; ++r) mk[r] = ((n >> 2) == r) ? msk : 0u;
    int off1 = MB1_OFF + (8 * cb + w) * 2048 + ((l >> 2) & 3) * 256 + (4 * q + (l & 3)) * 16, off1b = off1 + 1024;
    asm volatile("" : "+v"(mk[0]), "+v"(mk[1]), "+v"(mk[2]), "+v"(mk[3]), "+v"(off1), "+v"(off1b));
    __syncthreads();
    STAMP(3);
    if (w < 2) {
        const float g = (lds[tid] + lds[128 + tid]) + (lds[256 + tid] + lds[384 + tid]);
        const float u = (lds[512 + tid] + lds[640 + tid]) + (lds[768 + tid] + lds[896 + tid]);
        const v4u p = {f2u(g), tag, f2u(u), tag};
        const int off = MB1_OFF + (8 * cb + (tid >> 4)) * 2048 + kp * 256 + (tid & 15) * 16;
        __builtin_amdgcn_raw_buffer_store_b128(p, rs_ws, off, 0, AUX_SC1);
    }
    STAMP(4);
#define H1_WAIT 24
    if (H1_WAIT) __builtin_amdgcn_s_sleep(H1_WAIT);
    float p0, p1, p2, p3;
#define H1_SLP 0
    poll1<H1_SLP>(rs_ws, off1, off1b, tag, p0, p1, p2, p3);
    STAMP(5);
    unsigned hpk;
    {
        float g = p0 + p2, u = p1 + p3;
        g = dpp_add<DPP_ROR(4)>(g); u = dpp_add<DPP_ROR(4)>(u);
        g = dpp_add<DPP_ROR(8)>(g); u = dpp_add<DPP_ROR(8)>(u);
        g = rbf(g); u = rbf(u);
        const float t = g * fmaf(g * g, -0.10294323970f, -2.30220819814f);
        const float ge = rbf(g * __builtin_amdgcn_rcpf(1.0f + __builtin_amdgcn_exp2f(t)));
        const unsigned hb = f2bf(ge * u);
        hpk = hb | (hb << 16);
    }
    v4f o = {0.f, 0.f, 0.f, 0.f};
#define STEP(i) { const unsigned hh = (unsigned)__builtin_amdgcn_update_dpp(0, (int)hpk, DPP_QUAD(i), 0xf, 0xf, true); \
        const v4u a = {hh & mk[0], hh & mk[1], hh & mk[2], hh & mk[3]}; \
        o = __builtin_amdgcn_mfma_f32_16x16x32_bf16(__builtin_bit_cast(v8s, a), __builtin_bit_cast(v8s, dv[i]), o, 0, 0, 0); }
    STEP(0) STEP(1) STEP(2) STEP(3)
#undef STEP
    {
        v2f c; c[0] = o[0]; c[1] = o[1];
        *(v2f*)&lds[1024 + w * 128 + 8 * n + 2 * q] = c;
    }
    __syncthreads();
    STAMP(6);
    if (w == 0) {
        v2f s[8];
#pragma unroll
        for (int k = 0; k < 8; ++k) s[k] = *(const v2f*)&lds[1024 + k * 128 + 2 * l];
        const v2f t = ((s[0] + s[1]) + (s[2] + s[3])) + ((s[4] + s[5]) + (s[6] + s[7]));
        const v4u p = {f2u(t[0]), tag, f2u(t[1]), tag};
        const int off = MB2_OFF + (8 * kp + (l >> 3)) * RED_BYTES + cb * 128 + (l & 7) * 16;
        __builtin_amdgcn_raw_buffer_store_b128(p, rs_ws, off, 0, AUX_SC1);
    }
    STAMP(7);

    constexpr int FW = 0;
    if (b >= NRED || w != FW) { STAMP_FLUSH(); return; }
    {
        const int o0 = MB2_OFF + b * RED_BYTES + l * 16;
        float v[8];
        poll4(rs_ws, o0, l < 32 ? o0 + 3072 : o0, tag, v);
        STAMP(8);
        const float s0 = ((v[0] + v[2]) + v[4]) + (l < 32 ? v[6] : 0.f), s1 = ((v[1] + v[3]) + v[5]) + (l < 32 ? v[7] : 0.f);
        float s = swap32_add(s0, s1);
        s = swap16_add(s, s);
        s = dpp_add<DPP_ROR(8)>(s);
        if ((l & 24) == 0) __builtin_amdgcn_raw_buffer_store_b16((unsigned short)f2bf(s), rs_o, (16 * b + 2 * (l & 7) + (l >> 5)) * 2, 0, 0);
        if (b == 0 && l == 0) __hip_atomic_store((gu32*)ws, tag, RLX_AGENT);
    }
    STAMP(9);
    STAMP_FLUSH();
}

extern "C" void kernel_launch(void* const* d_in, const int* in_sizes, int n_in,
                              void* d_out, int out_size, void* d_ws, size_t ws_size,
                              hipStream_t stream) {
    geglu_decode_kernel<<<NWG, 512, 0, stream>>>((const unsigned short*)d_in[0], (const unsigned short*)d_in[1],
        (const unsigned short*)d_in[2], (const unsigned short*)d_in[3], (unsigned short*)d_out, (unsigned char*)d_ws);
}
```

```cpp
#include <hip/hip_runtime.h>
#include <hip/hip_bf16.h>
#include <stdint.h>

typedef unsigned v4u __attribute__((ext_vector_type(4)));
typedef float v4f __attribute__((ext_vector_type(4)));
typedef float v2f __attribute__((ext_vector_type(2)));
typedef short v8s __attribute__((ext_vector_type(8)));
#define GAS __attribute__((address_space(1)))
typedef GAS unsigned gu32;
#define RLX_AGENT __ATOMIC_RELAXED, __HIP_MEMORY_SCOPE_AGENT

constexpr int DM = 1024, DF = 3584;
constexpr int NWG = 224;
constexpr int NGRP = 28;
constexpr int NXCD = 8, PER_XCD = NWG / NXCD;
constexpr int NRED = 64;
constexpr int MB1_OFF = 4096;
constexpr int MB1_BYTES = NWG * 2048;
constexpr int MB2_OFF = MB1_OFF + MB1_BYTES;
constexpr int RED_BYTES = 4096;
constexpr int MB2_BYTES = NRED * RED_BYTES;
constexpr int WS_BYTES = MB2_OFF + MB2_BYTES;
constexpr int AUX_NT = 2, AUX_SC1 = 16;
constexpr int RSRC_W3 = 0x00020000;

__device__ __forceinline__ float u2f(unsigned v) { return __builtin_bit_cast(float, v); }
__device__ __forceinline__ unsigned f2u(float v) { return __builtin_bit_cast(unsigned, v); }
__device__ __forceinline__ float bf_lo(unsigned v) { return __builtin_bit_cast(float, v << 16); }
__device__ __forceinline__ float bf_hi(unsigned v) { return __builtin_bit_cast(float, v & 0xffff0000u); }
__device__ __forceinline__ unsigned f2bf(float f) {
    __hip_bfloat16 b = __float2bfloat16(f);
    return (unsigned)__builtin_bit_cast(unsigned short, b);
}
__device__ __forceinline__ float rbf(float f) { return __builtin_bit_cast(float, f2bf(f) << 16); }

template <int CTRL> __device__ __forceinline__ float dpp_add(float v) {
    int t = __builtin_amdgcn_update_dpp(0, __builtin_bit_cast(int, v), CTRL, 0xf, 0xf, true);
    return v + __builtin_bit_cast(float, t);
}
__device__ __forceinline__ float swap32_add(float a, float b) {
    auto r = __builtin_amdgcn_permlane32_swap(f2u(a), f2u(b), false, false);
    return u2f(r[0]) + u2f(r[1]);
}
__device__ __forceinline__ float swap16_add(float a, float b) {
    auto r = __builtin_amdgcn_permlane16_swap(f2u(a), f2u(b), false, false);
    return u2f(r[0]) + u2f(r[1]);
}
#define STAMP(k)
#define STAMP_DECL
#define STAMP_FLUSH()
constexpr int DPP_ROR(int n) { return 0x120 + n; }
constexpr int DPP_QUAD(int i) { return i | (i << 2) | (i << 4) | (i << 6); }
#define PREWARM 0
#define PREWRITE 2
#define P4_SLEEP "s_sleep 2\n\t"
#define H2_D2 1
#define NE 9
constexpr int NL = 16 - NE;
constexpr int OOB = 0x40000000;
#define WD_DELAY 8

#define POLL_ISSUE(R0, R1) \
    "buffer_load_dwordx4 " R0 ", %[voff], %[rs], 0 offen sc1\n\t" \
    "buffer_load_dwordx4 " R1 ", %[voff2], %[rs], 0 offen sc1\n\t"
#define POLL_CHECK(X0, Y0, Z0, W0, X1, Y1, Z1, W1) \
    "s_waitcnt vmcnt(4)\n\t" \
    "v_cmp_ne_u32_e32 vcc, %[tag], " Y0 "\n\t" \
    "v_cmp_ne_u32_e64 %[t0], %[tag], " W0 "\n\t" \
    "v_cmp_ne_u32_e64 %[t1], %[tag], " Y1 "\n\t" \
    "v_cmp_ne_u32_e64 %[t2], %[tag], " W1 "\n\t" \
    "s_or_b64 vcc, vcc, %[t0]\n\t" \
    "s_or_b64 %[t1], %[t1], %[t2]\n\t" \
    "s_or_b64 %[t0], vcc, %[t1]\n\t" \
    "s_andn2_b64 exec, exec, %[t0]\n\t" \
    "v_mov_b32 %[r0], " X0 "\n\t" \
    "v_mov_b32 %[r1], " Z0 "\n\t" \
    "v_mov_b32 %[r2], " X1 "\n\t" \
    "v_mov_b32 %[r3], " Z1 "\n\t" \
    "s_mov_b64 exec, %[t0]\n\t" \
    "s_cmp_eq_u64 %[t0], 0\n\t" \
    "s_cbranch_scc1 2f\n\t"
#define POLL_BODY(SKIP) \
    asm volatile( \
        "s_cmp_eq_u32 %[sel], 0\n\t" \
        SKIP " 3f\n\t" \
        "s_mov_b64 %[save], exec\n\t" \
        "s_mov_b32 %[cnt], 0x100000\n\t" \
        "s_nop 3\n\t" \
        POLL_ISSUE("v[104:107]", "v[108:111]") \
        POLL_ISSUE("v[112:115]", "v[116:119]") \
        "1:\n\t" \
        POLL_ISSUE("v[120:123]", "v[124:127]") \
        POLL_CHECK("v104", "v105", "v106", "v107", "v108", "v109", "v110", "v111") \
        POLL_ISSUE("v[104:107]", "v[108:111]") \
        POLL_CHECK("v112", "v113", "v114", "v115", "v116", "v117", "v118", "v119") \
        POLL_ISSUE("v[112:115]", "v[116:119]") \
        POLL_CHECK("v120", "v121", "v122", "v123", "v124", "v125", "v126", "v127") \
        "s_sub_u32 %[cnt], %[cnt], 1\n\t" \
        "s_cbranch_scc0 1b\n\t" \
        "2:\n\t" \
        "s_mov_b64 exec, %[save]\n\t" \
        "3:\n\t" \
        : [r0] "=&v"(r0), [r1] "=&v"(r1), [r2] "=&v"(r2), [r3] "=&v"(r3), [save] "=&s"(save), [t0] "=&s"(t0), [t1] "=&s"(t1), [t2] "=&s"(t2), [cnt] "=&s"(cnt) \
        : [voff] "v"(voff), [voff2] "v"(voff2), [rs] "s"(rs), [tag] "s"(tag), [sel] "s"(sel) \
        : "memory", "vcc", "scc", \
          "v104", "v105", "v106", "v107", "v108", "v109", "v110", "v111", "v112", "v113", "v114", "v115", \
          "v116", "v117", "v118", "v119", "v120", "v121", "v122", "v123", "v124", "v125", "v126", "v127")
#define POLL1_BODY(SLEEP) \
    asm volatile( \
        "s_mov_b64 %[save], exec\n\t" \
        "s_mov_b32 %[cnt], 0x100000\n\t" \
        "s_nop 3\n\t" \
        "1:\n\t" \
        POLL_ISSUE("v[104:107]", "v[108:111]") \
        "s_waitcnt vmcnt(0)\n\t" \
        "v_cmp_ne_u32_e32 vcc, %[tag], v105\n\t" \
        "v_cmp_ne_u32_e64 %[t0], %[tag], v107\n\t" \
        "v_cmp_ne_u32_e64 %[t1], %[tag], v109\n\t" \
        "v_cmp_ne_u32_e64 %[t2], %[tag], v111\n\t" \
        "s_or_b64 vcc, vcc, %[t0]\n\t" \
        "s_or_b64 %[t1], %[t1], %[t2]\n\t" \
        "s_or_b64 exec, vcc, %[t1]\n\t" \
        "s_cbranch_execz 2f\n\t" \
        SLEEP \
        "s_sub_u32 %[cnt], %[cnt], 1\n\t" \
        "s_cbranch_scc0 1b\n\t" \
        "2:\n\t" \
        "s_mov_b64 exec, %[save]\n\t" \
        "v_mov_b32 %[r0], v104\n\t" \
        "v_mov_b32 %[r1], v106\n\t" \
        "v_mov_b32 %[r2], v108\n\t" \
        "v_mov_b32 %[r3], v110\n\t" \
        : [r0] "=&v"(r0), [r1] "=&v"(r1), [r2] "=&v"(r2), [r3] "=&v"(r3), [save] "=&s"(save), [t0] "=&s"(t0), [t1] "=&s"(t1), [t2] "=&s"(t2), [cnt] "=&s"(cnt) \
        : [voff] "v"(voff), [voff2] "v"(voff2), [rs] "s"(rs), [tag] "s"(tag) \
        : "memory", "vcc", "scc", "v104", "v105", "v106", "v107", "v108", "v109", "v110", "v111")
template <int SLP>
__device__ __forceinline__ void poll1(__amdgpu_buffer_rsrc_t rs, int voff, int voff2, unsigned tag, float& r0, float& r1, float& r2, float& r3)
{
    unsigned long long save, t0, t1, t2; unsigned cnt;
    if constexpr (SLP == 0) { POLL1_BODY(""); } else if constexpr (SLP == 1) { POLL1_BODY("s_sleep 2\n\t"); } else { POLL1_BODY("s_sleep 6\n\t"); }
}
__device__ __forceinline__ void poll4(__amdgpu_buffer_rsrc_t rs, int voff, int voff4, unsigned tag, float (&r)[8])
{
    unsigned long long save, t0, t1, t2; unsigned cnt;
    asm volatile(
        "s_mov_b64 %[save], exec\n\t"
        "s_mov_b32 %[cnt], 0x100000\n\t"
        "s_nop 3\n\t"
        "1:\n\t"
        "buffer_load_dwordx4 v[104:107], %[voff], %[rs], 0 offen sc1\n\t"
        "buffer_load_dwordx4 v[108:111], %[voff], %[rs], 0 offen offset:1024 sc1\n\t"
        "buffer_load_dwordx4 v[112:115], %[voff], %[rs], 0 offen offset:2048 sc1\n\t"
        "buffer_load_dwordx4 v[116:119], %[voff4], %[rs], 0 offen sc1\n\t"
        "s_waitcnt vmcnt(2)\n\t"
        "v_cmp_ne_u32_e32 vcc, %[tag], v105\n\t"
        "v_cmp_ne_u32_e64 %[t0], %[tag], v107\n\t"
        "v_cmp_ne_u32_e64 %[t1], %[tag], v109\n\t"
        "v_cmp_ne_u32_e64 %[t2], %[tag], v111\n\t"
        "s_or_b64 vcc, vcc, %[t0]\n\t"
        "s_or_b64 %[t1], %[t1], %[t2]\n\t"
        "s_or_b64 %[t0], vcc, %[t1]\n\t"
        "s_waitcnt vmcnt(0)\n\t"
        "v_cmp_ne_u32_e32 vcc, %[tag], v113\n\t"
        "v_cmp_ne_u32_e64 %[t1], %[tag], v115\n\t"
        "s_or_b64 %[t0], %[t0], vcc\n\t"
        "v_cmp_ne_u32_e32 vcc, %[tag], v117\n\t"
        "v_cmp_ne_u32_e64 %[t2], %[tag], v119\n\t"
        "s_or_b64 %[t0], %[t0], %[t1]\n\t"
        "s_or_b64 vcc, vcc, %[t2]\n\t"
        "s_or_b64 exec, vcc, %[t0]\n\t"
        "s_cbranch_execz 2f\n\t"
        "s_sub_u32 %[cnt], %[cnt], 1\n\t"
        "s_cbranch_scc0 1b\n\t"
        "2:\n\t"
        "s_mov_b64 exec, %[save]\n\t"
        "v_mov_b32 %[r0], v104\n\t"
        "v_mov_b32 %[r1], v106\n\t"
        "v_mov_b32 %[r2], v108\n\t"
        "v_mov_b32 %[r3], v110\n\t"
        "v_mov_b32 %[r4], v112\n\t"
        "v_mov_b32 %[r5], v114\n\t"
        "v_mov_b32 %[r6], v116\n\t"
        "v_mov_b32 %[r7], v118\n\t"
        : [r0] "=&v"(r[0]), [r1] "=&v"(r[1]), [r2] "=&v"(r[2]), [r3] "=&v"(r[3]), [r4] "=&v"(r[4]), [r5] "=&v"(r[5]), [r6] "=&v"(r[6]), [r7] "=&v"(r[7]),
          [save] "=&s"(save), [t0] "=&s"(t0), [t1] "=&s"(t1), [t2] "=&s"(t2), [cnt] "=&s"(cnt)
        : [voff] "v"(voff), [voff4] "v"(voff4), [rs] "s"(rs), [tag] "s"(tag)
        : "memory", "vcc", "scc", "v104", "v105", "v106", "v107", "v108", "v109", "v110", "v111", "v112", "v113", "v114", "v115", "v116", "v117", "v118", "v119");
}
#define P4_ISSUE(B) \
        "buffer_load_dwordx4 v[" #B "+0:" #B "+3], %[voff], %[rs], 0 offen sc1\n\t" \
        "buffer_load_dwordx4 v[" #B "+4:" #B "+7], %[voff], %[rs], 0 offen offset:1024 sc1\n\t" \
        "buffer_load_dwordx4 v[" #B "+8:" #B "+11], %[voff], %[rs], 0 offen offset:2048 sc1\n\t" \
        "buffer_load_dwordx4 v[" #B "+12:" #B "+15], %[voff4], %[rs], 0 offen sc1\n\t"
#define P4_CHECK(B) \
        "s_waitcnt vmcnt(4)\n\t" \
        "v_cmp_ne_u32_e32 vcc, %[tag], v[" #B "+1]\n\t" \
        "v_cmp_ne_u32_e64 %[t0], %[tag], v[" #B "+3]\n\t" \
        "v_cmp_ne_u32_e64 %[t1], %[tag], v[" #B "+5]\n\t" \
        "v_cmp_ne_u32_e64 %[t2], %[tag], v[" #B "+7]\n\t" \
        "s_or_b64 vcc, vcc, %[t0]\n\t" \
        "s_or_b64 %[t1], %[t1], %[t2]\n\t" \
        "s_or_b64 %[t0], vcc, %[t1]\n\t" \
        "v_cmp_ne_u32_e32 vcc, %[tag], v[" #B "+9]\n\t" \
        "v_cmp_ne_u32_e64 %[t1], %[tag], v[" #B "+11]\n\t" \
        "s_or_b64 %[t0], %[t0], vcc\n\t" \
        "v_cmp_ne_u32_e32 vcc, %[tag], v[" #B "+13]\n\t" \
        "v_cmp_ne_u32_e64 %[t2], %[tag], v[" #B "+15]\n\t" \
        "s_or_b64 %[t0], %[t0], %[t1]\n\t" \
        "s_or_b64 vcc, vcc, %[t2]\n\t" \
        "s_or_b64 %[t0], vcc, %[t0]\n\t" \
        "s_andn2_b64 exec, exec, %[t0]\n\t" \
        "v_mov_b32 %[r0], v[" #B "+0]\n\t" \
        "v_mov_b32 %[r1], v[" #B "+2]\n\t" \
        "v_mov_b32 %[r2], v[" #B "+4]\n\t" \
        "v_mov_b32 %[r3], v[" #B "+6]\n\t" \
        "v_mov_b32 %[r4], v[" #B "+8]\n\t" \
        "v_mov_b32 %[r5], v[" #B "+10]\n\t" \
        "v_mov_b32 %[r6], v[" #B "+12]\n\t" \
        "v_mov_b32 %[r7], v[" #B "+14]\n\t" \
        "s_mov_b64 exec, %[t0]\n\t" \
        "s_cmp_eq_u64 %[t0], 0\n\t" \
        "s_cbranch_scc1 2f\n\t"
__device__ __forceinline__ void poll4x2(__amdgpu_buffer_rsrc_t rs, int voff, int voff4, unsigned tag, float (&r)[8])
{
    unsigned long long save, t0, t1, t2; unsigned cnt;
    asm volatile(
        "s_mov_b64 %[save], exec\n\t"
        "s_mov_b32 %[cnt], 0x100000\n\t"
        "s_nop 3\n\t"
        P4_ISSUE(104)
        "1:\n\t"
        P4_SLEEP
        P4_ISSUE(120)
        P4_CHECK(104)
        P4_SLEEP
        P4_ISSUE(104)
        P4_CHECK(120)
        "s_sub_u32 %[cnt], %[cnt], 1\n\t"
        "s_cbranch_scc0 1b\n\t"
        "2:\n\t"
        "s_mov_b64 exec, %[save]\n\t"
        : [r0] "=&v"(r[0]), [r1] "=&v"(r[1]), [r2] "=&v"(r[2]), [r3] "=&v"(r[3]), [r4] "=&v"(r[4]), [r5] "=&v"(r[5]), [r6] "=&v"(r[6]), [r7] "=&v"(r[7]),
          [save] "=&s"(save), [t0] "=&s"(t0), [t1] "=&s"(t1), [t2] "=&s"(t2), [cnt] "=&s"(cnt)
        : [voff] "v"(voff), [voff4] "v"(voff4), [rs] "s"(rs), [tag] "s"(tag)
        : "memory", "vcc", "scc", "v104", "v105", "v106", "v107", "v108", "v109", "v110", "v111", "v112", "v113", "v114", "v115", "v116", "v117", "v118", "v119",
          "v120", "v121", "v122", "v123", "v124", "v125", "v126", "v127", "v128", "v129", "v130", "v131", "v132", "v133", "v134", "v135");
}
__device__ __forceinline__ void poll2(__amdgpu_buffer_rsrc_t rs, int voff, int voff2, unsigned tag, int sel, float& r0, float& r1, float& r2, float& r3)
{
    unsigned long long save, t0, t1, t2; unsigned cnt;
    POLL_BODY("s_cbranch_scc0");
}

__global__ __launch_bounds__(512, 2) void geglu_decode_kernel(
    const unsigned short* __restrict__ x, const unsigned short* __restrict__ wg, const unsigned short* __restrict__ wu,
    const unsigned short* __restrict__ wd, unsigned short* out, unsigned char* ws)
{
    STAMP_DECL
    STAMP(0);
    __shared__ __attribute__((aligned(16))) float lds[2048];
    const int tid = threadIdx.x, l = tid & 63;
    const int w = __builtin_amdgcn_readfirstlane(tid >> 6);
    const int b = blockIdx.x;
    const int cb = b >> 3, kp = b & 7;

    const int mat = w >> 2, ww = w & 3;
    const __amdgpu_buffer_rsrc_t rs_gu = __builtin_amdgcn_make_buffer_rsrc((void*)(mat ? wu : wg), 0, DM * DF * 2, RSRC_W3);
    const __amdgpu_buffer_rsrc_t rs_d = __builtin_amdgcn_make_buffer_rsrc((void*)wd, 0, DM * DF * 2, RSRC_W3);
    const __amdgpu_buffer_rsrc_t rs_x = __builtin_amdgcn_make_buffer_rsrc((void*)x, 0, DM * 2, RSRC_W3);
    const __amdgpu_buffer_rsrc_t rs_ws = __builtin_amdgcn_make_buffer_rsrc((void*)ws, 0, WS_BYTES, RSRC_W3);
    const __amdgpu_buffer_rsrc_t rs_o = __builtin_amdgcn_make_buffer_rsrc((void*)out, 0, DM * 2, RSRC_W3);

    unsigned ep;
    asm volatile("s_load_dword %0, %1, 0x0" : "=s"(ep) : "s"(ws), "s"(x), "s"(wg), "s"(wu), "s"(wd), "s"(out) : "memory");
    const int q = l >> 4, n = l & 15;
    const bool late = kp >= 4;
    const int base = late ? 64 * NE + 16 * NL * (kp - 4) : 16 * NE * kp;
    const int row0 = base + 4 * ww + q;
    const int voff = row0 * (DF * 2) + (128 * cb + 8 * n) * 2;
    const int voff2 = late ? OOB : voff;
    v4u wv[NE];
    wv[0] = __builtin_amdgcn_raw_buffer_load_b128(rs_gu, voff, 0, AUX_NT);
    const unsigned xs = __builtin_amdgcn_raw_buffer_load_b16(rs_x, (n < (late ? NL : NE)) ? (row0 + 16 * n) * 2 : OOB, 0, 0);
#pragma unroll
    for (int i = 1; i < NE; ++i) wv[i] = __builtin_amdgcn_raw_buffer_load_b128(rs_gu, i < NL ? voff : voff2, i * 16 * (DF * 2), AUX_NT);
    const int doff = (128 * cb + 16 * w + 4 * q) * (DM * 2) + (128 * kp + 8 * n) * 2;
    v4u dv[4];
    const int pub1 = MB1_OFF + (8 * cb + (tid >> 4)) * 2048 + kp * 256 + (tid & 15) * 16;
    const int pub2 = MB2_OFF + (8 * kp + (l >> 3)) * RED_BYTES + cb * 128 + (l & 7) * 16;
    __builtin_amdgcn_sched_barrier(0);
    asm volatile("s_waitcnt lgkmcnt(0)" : "+s"(ep) :: "memory");
    const unsigned tag = ep * 1664525u + 1013904223u;
    if (w < 2) {
        const v4u z = {0u, ~tag, 0u, ~tag};
        __builtin_amdgcn_raw_buffer_store_b128(z, rs_ws, pub1, 0, AUX_SC1);
        if (PREWRITE > 1 && w == 0) __builtin_amdgcn_raw_buffer_store_b128(z, rs_ws, pub2, 0, AUX_SC1);
    }
    __builtin_amdgcn_sched_barrier(0);
    STAMP(1);

    float acc[8];
#pragma unroll
    for (int k = 0; k < 8; ++k) acc[k] = 0.f;
    const int xf = (int)(xs << 16);
#define FMA_UNIT(i) if constexpr ((i) < NE) { \
        const float xi = __builtin_bit_cast(float, __builtin_amdgcn_update_dpp(0, xf, 0x150 + (i), 0xf, 0xf, false));   \
        _Pragma("unroll") for (int k = 0; k < 4; ++k) { \
            acc[2 * k] = fmaf(xi, bf_lo(wv[i][k]), acc[2 * k]); \
            acc[2 * k + 1] = fmaf(xi, bf_hi(wv[i][k]), acc[2 * k + 1]); } }
    FMA_UNIT(0) FMA_UNIT(1) FMA_UNIT(2) FMA_UNIT(3) FMA_UNIT(4) FMA_UNIT(5) FMA_UNIT(6) FMA_UNIT(7) FMA_UNIT(8) FMA_UNIT(9) FMA_UNIT(10) FMA_UNIT(11)
#undef FMA_UNIT
    asm volatile("" :: "v"(wv[NE - 1]));
    __builtin_amdgcn_sched_barrier(0);
#pragma unroll
    for (int k = 0; k < 4; ++k) dv[k] = __builtin_amdgcn_raw_buffer_load_b128(rs_d, doff, k * (DM * 2), AUX_NT);
    __builtin_amdgcn_sched_barrier(0);
    STAMP(2);
    {
        const float b0 = swap32_add(acc[0], acc[4]), b1 = swap32_add(acc[1], acc[5]);
        const float b2 = swap32_add(acc[2], acc[6]), b3 = swap32_add(acc[3], acc[7]);
        v2f c; c[0] = swap16_add(b0, b2); c[1] = swap16_add(b1, b3);
        *(v2f*)&lds[w * 128 + 8 * n + 2 * q] = c;
    }
    const unsigned msk = ((n & 3) < 2) ? ((n & 1) ? 0xffff0000u : 0x0000ffffu) : 0u;
    unsigned mk[4];
#pragma unroll
    for (int r = 0; r < 4; ++r) mk[r] = ((n >> 2) == r) ? msk : 0u;
    int off1 = MB1_OFF + (8 * cb + w) * 2048 + ((l >> 2) & 3) * 256 + (4 * q + (l & 3)) * 16, off1b = off1 + 1024;
    asm volatile("" : "+v"(mk[0]), "+v"(mk[1]), "+v"(mk[2]), "+v"(mk[3]), "+v"(off1), "+v"(off1b));
    __syncthreads();
    STAMP(3);
    if (w < 2) {
        const float g = (lds[tid] + lds[128 + tid]) + (lds[256 + tid] + lds[384 + tid]);
        const float u = (lds[512 + tid] + lds[640 + tid]) + (lds[768 + tid] + lds[896 + tid]);
        const v4u p = {f2u(g), tag, f2u(u), tag};
        asm volatile("s_waitcnt vmcnt(4)" ::: "memory");
        __builtin_amdgcn_raw_buffer_store_b128(p, rs_ws, pub1, 0, AUX_SC1);
    }
    STAMP(4);
#define H1_WAIT 14
    if (H1_WAIT) __builtin_amdgcn_s_sleep(H1_WAIT);
    float p0, p1, p2, p3;
#define H1_SLP 0
    poll1<H1_SLP>(rs_ws, off1, off1b, tag, p0, p1, p2, p3);
    STAMP(5);
    unsigned hpk;
    {
        float g = p0 + p2, u = p1 + p3;
        g = dpp_add<DPP_ROR(4)>(g); u = dpp_add<DPP_ROR(4)>(u);
        g = dpp_add<DPP_ROR(8)>(g); u = dpp_add<DPP_ROR(8)>(u);
        g = rbf(g); u = rbf(u);
        const float t = g * fmaf(g * g, -0.10294323970f, -2.30220819814f);
        const float ge = rbf(g * __builtin_amdgcn_rcpf(1.0f + __builtin_amdgcn_exp2f(t)));
        const unsigned hb = f2bf(ge * u);
        hpk = hb | (hb << 16);
    }
    v4f o = {0.f, 0.f, 0.f, 0.f};
#define STEP(i) { const unsigned hh = (unsigned)__builtin_amdgcn_update_dpp(0, (int)hpk, DPP_QUAD(i), 0xf, 0xf, true); \
        const v4u a = {hh & mk[0], hh & mk[1], hh & mk[2], hh & mk[3]}; \
        o = __builtin_amdgcn_mfma_f32_16x16x32_bf16(__builtin_bit_cast(v8s, a), __builtin_bit_cast(v8s, dv[i]), o, 0, 0, 0); }
    STEP(0) STEP(1) STEP(2) STEP(3)
#undef STEP
    {
        v2f c; c[0] = o[0]; c[1] = o[1];
        *(v2f*)&lds[1024 + w * 128 + 8 * n + 2 * q] = c;
    }
    __syncthreads();
    STAMP(6);
    if (w == 0) {
        v2f s[8];
#pragma unroll
        for (int k = 0; k < 8; ++k) s[k] = *(const v2f*)&lds[1024 + k * 128 + 2 * l];
        const v2f t = ((s[0] + s[1]) + (s[2] + s[3])) + ((s[4] + s[5]) + (s[6] + s[7]));
        const v4u p = {f2u(t[0]), tag, f2u(t[1]), tag};
        __builtin_amdgcn_raw_buffer_store_b128(p, rs_ws, pub2, 0, AUX_SC1);
    }
    STAMP(7);

    constexpr int FW = 0;
    if (b >= NRED || w != FW) { STAMP_FLUSH(); return; }
    {
        const int o0 = MB2_OFF + b * RED_BYTES + l * 16;
        float v[8];
        poll4x2(rs_ws, o0, l < 32 ? o0 + 3072 : o0, tag, v);
        STAMP(8);
        const float s0 = ((v[0] + v[2]) + v[4]) + (l < 32 ? v[6] : 0.f), s1 = ((v[1] + v[3]) + v[5]) + (l < 32 ? v[7] : 0.f);
        float s = swap32_add(s0, s1);
        s = swap16_add(s, s);
        s = dpp_add<DPP_ROR(8)>(s);
        if ((l & 24) == 0) __builtin_amdgcn_raw_buffer_store_b16((unsigned short)f2bf(s), rs_o, (16 * b + 2 * (l & 7) + (l >> 5)) * 2, 0, 0);
        if (b == 0 && l == 0) __hip_atomic_store((gu32*)ws, tag, RLX_AGENT);
    }
    STAMP(9);
    STAMP_FLUSH();
}

extern "C" void kernel_launch(void* const* d_in, const int* in_sizes, int n_in,
                              void* d_out, int out_size, void* d_ws, size_t ws_size,
                              hipStream_t stream) {
    geglu_decode_kernel<<<NWG, 512, 0, stream>>>((const unsigned short*)d_in[0], (const unsigned short*)d_in[1],
        (const unsigned short*)d_in[2], (const unsigned short*)d_in[3], (unsigned short*)d_out, (unsigned char*)d_ws);
}
```

```cpp
#include <hip/hip_runtime.h>
#include <hip/hip_bf16.h>
#include <stdint.h>

typedef unsigned v4u __attribute__((ext_vector_type(4)));
typedef float v4f __attribute__((ext_vector_type(4)));
typedef float v2f __attribute__((ext_vector_type(2)));
typedef short v8s __attribute__((ext_vector_type(8)));
#define GAS __attribute__((address_space(1)))
typedef GAS unsigned gu32;
#define RLX_AGENT __ATOMIC_RELAXED, __HIP_MEMORY_SCOPE_AGENT

constexpr int DM = 1024, DF = 3584;
constexpr int NWG = 224;
constexpr int NGRP = 28;
constexpr int NXCD = 8, PER_XCD = NWG / NXCD;
constexpr int NRED = 64;
constexpr int MB1_OFF = 4096;
constexpr int MB1_BYTES = NWG * 2048;
constexpr int MB2_OFF = MB1_OFF + MB1_BYTES;
constexpr int RED_BYTES = 4096;
constexpr int MB2_BYTES = NRED * RED_BYTES;
constexpr int WS_BYTES = MB2_OFF + MB2_BYTES;
constexpr int AUX_NT = 2, AUX_SC1 = 16;
constexpr int RSRC_W3 = 0x00020000;

__device__ __forceinline__ float u2f(unsigned v) { return __builtin_bit_cast(float, v); }
__device__ __forceinline__ unsigned f2u(float v) { return __builtin_bit_cast(unsigned, v); }
__device__ __forceinline__ float bf_lo(unsigned v) { return __builtin_bit_cast(float, v << 16); }
__device__ __forceinline__ float bf_hi(unsigned v) { return __builtin_bit_cast(float, v & 0xffff0000u); }
__device__ __forceinline__ unsigned f2bf(float f) {
    __hip_bfloat16 b = __float2bfloat16(f);
    return (unsigned)__builtin_bit_cast(unsigned short, b);
}
__device__ __forceinline__ float rbf(float f) { return __builtin_bit_cast(float, f2bf(f) << 16); }

template <int CTRL> __device__ __forceinline__ float dpp_add(float v) {
    int t = __builtin_amdgcn_update_dpp(0, __builtin_bit_cast(int, v), CTRL, 0xf, 0xf, true);
    return v + __builtin_bit_cast(float, t);
}
__device__ __forceinline__ float swap32_add(float a, float b) {
    auto r = __builtin_amdgcn_permlane32_swap(f2u(a), f2u(b), false, false);
    return u2f(r[0]) + u2f(r[1]);
}
__device__ __forceinline__ float swap16_add(float a, float b) {
    auto r = __builtin_amdgcn_permlane16_swap(f2u(a), f2u(b), false, false);
    return u2f(r[0]) + u2f(r[1]);
}
#define STAMP(k)
#define STAMP_DECL
#define STAMP_FLUSH()
constexpr int DPP_ROR(int n) { return 0x120 + n; }
constexpr int DPP_QUAD(int i) { return i | (i << 2) | (i << 4) | (i << 6); }
#define PREWARM 0
#define PREWRITE 2
#define P4_SLEEP "s_sleep 2\n\t"
#define H2_D2 1
#define NE 9
constexpr int NL = 16 - NE;
constexpr int OOB = 0x40000000;
#define WD_DELAY 8

#define POLL_ISSUE(R0, R1) \
    "buffer_load_dwordx4 " R0 ", %[voff], %[rs], 0 offen sc1\n\t" \
    "buffer_load_dwordx4 " R1 ", %[voff2], %[rs], 0 offen sc1\n\t"
#define POLL_CHECK(X0, Y0, Z0, W0, X1, Y1, Z1, W1) \
    "s_waitcnt vmcnt(4)\n\t" \
    "v_cmp_ne_u32_e32 vcc, %[tag], " Y0 "\n\t" \
    "v_cmp_ne_u32_e64 %[t0], %[tag], " W0 "\n\t" \
    "v_cmp_ne_u32_e64 %[t1], %[tag], " Y1 "\n\t" \
    "v_cmp_ne_u32_e64 %[t2], %[tag], " W1 "\n\t" \
    "s_or_b64 vcc, vcc, %[t0]\n\t" \
    "s_or_b64 %[t1], %[t1], %[t2]\n\t" \
    "s_or_b64 %[t0], vcc, %[t1]\n\t" \
    "s_andn2_b64 exec, exec, %[t0]\n\t" \
    "v_mov_b32 %[r0], " X0 "\n\t" \
    "v_mov_b32 %[r1], " Z0 "\n\t" \
    "v_mov_b32 %[r2], " X1 "\n\t" \
    "v_mov_b32 %[r3], " Z1 "\n\t" \
    "s_mov_b64 exec, %[t0]\n\t" \
    "s_cmp_eq_u64 %[t0], 0\n\t" \
    "s_cbranch_scc1 2f\n\t"
#define POLL_BODY(SKIP) \
    asm volatile( \
        "s_cmp_eq_u32 %[sel], 0\n\t" \
        SKIP " 3f\n\t" \
        "s_mov_b64 %[save], exec\n\t" \
        "s_mov_b32 %[cnt], 0x100000\n\t" \
        "s_nop 3\n\t" \
        POLL_ISSUE("v[104:107]", "v[108:111]") \
        POLL_ISSUE("v[112:115]", "v[116:119]") \
        "1:\n\t" \
        POLL_ISSUE("v[120:123]", "v[124:127]") \
        POLL_CHECK("v104", "v105", "v106", "v107", "v108", "v109", "v110", "v111") \
        POLL_ISSUE("v[104:107]", "v[108:111]") \
        POLL_CHECK("v112", "v113", "v114", "v115", "v116", "v117", "v118", "v119") \
        POLL_ISSUE("v[112:115]", "v[116:119]") \
        POLL_CHECK("v120", "v121", "v122", "v123", "v124", "v125", "v126", "v127") \
        "s_sub_u32 %[cnt], %[cnt], 1\n\t" \
        "s_cbranch_scc0 1b\n\t" \
        "2:\n\t" \
        "s_mov_b64 exec, %[save]\n\t" \
        "3:\n\t" \
        : [r0] "=&v"(r0), [r1] "=&v"(r1), [r2] "=&v"(r2), [r3] "=&v"(r3), [save] "=&s"(save), [t0] "=&s"(t0), [t1] "=&s"(t1), [t2] "=&s"(t2), [cnt] "=&s"(cnt) \
        : [voff] "v"(voff), [voff2] "v"(voff2), [rs] "s"(rs), [tag] "s"(tag), [sel] "s"(sel) \
        : "memory", "vcc", "scc", \
          "v104", "v105", "v106", "v107", "v108", "v109", "v110", "v111", "v112", "v113", "v114", "v115", \
          "v116", "v117", "v118", "v119", "v120", "v121", "v122", "v123", "v124", "v125", "v126", "v127")
#define POLL1_BODY(SLEEP) \
    asm volatile( \
        "s_mov_b64 %[save], exec\n\t" \
        "s_mov_b32 %[cnt], 0x100000\n\t" \
        "s_nop 3\n\t" \
        "1:\n\t" \
        POLL_ISSUE("v[104:107]", "v[108:111]") \
        "s_waitcnt vmcnt(0)\n\t" \
        "v_cmp_ne_u32_e32 vcc, %[tag], v105\n\t" \
        "v_cmp_ne_u32_e64 %[t0], %[tag], v107\n\t" \
        "v_cmp_ne_u32_e64 %[t1], %[tag], v109\n\t" \
        "v_cmp_ne_u32_e64 %[t2], %[tag], v111\n\t" \
        "s_or_b64 vcc, vcc, %[t0]\n\t" \
        "s_or_b64 %[t1], %[t1], %[t2]\n\t" \
        "s_or_b64 exec, vcc, %[t1]\n\t" \
        "s_cbranch_execz 2f\n\t" \
        SLEEP \
        "s_sub_u32 %[cnt], %[cnt], 1\n\t" \
        "s_cbranch_scc0 1b\n\t" \
        "2:\n\t" \
        "s_mov_b64 exec, %[save]\n\t" \
        "v_mov_b32 %[r0], v104\n\t" \
        "v_mov_b32 %[r1], v106\n\t" \
        "v_mov_b32 %[r2], v108\n\t" \
        "v_mov_b32 %[r3], v110\n\t" \
        : [r0] "=&v"(r0), [r1] "=&v"(r1), [r2] "=&v"(r2), [r3] "=&v"(r3), [save] "=&s"(save), [t0] "=&s"(t0), [t1] "=&s"(t1), [t2] "=&s"(t2), [cnt] "=&s"(cnt) \
        : [voff] "v"(voff), [voff2] "v"(voff2), [rs] "s"(rs), [tag] "s"(tag) \
        : "memory", "vcc", "scc", "v104", "v105", "v106", "v107", "v108", "v109", "v110", "v111")
template <int SLP>
__device__ __forceinline__ void poll1(__amdgpu_buffer_rsrc_t rs, int voff, int voff2, unsigned tag, float& r0, float& r1, float& r2, float& r3)
{
    unsigned long long save, t0, t1, t2; unsigned cnt;
    if constexpr (SLP == 0) { POLL1_BODY(""); } else if constexpr (SLP == 1) { POLL1_BODY("s_sleep 2\n\t"); } else { POLL1_BODY("s_sleep 6\n\t"); }
}
__device__ __forceinline__ void poll4(__amdgpu_buffer_rsrc_t rs, int voff, int voff4, unsigned tag, float (&r)[8])
{
    unsigned long long save, t0, t1, t2; unsigned cnt;
    asm volatile(
        "s_mov_b64 %[save], exec\n\t"
        "s_mov_b32 %[cnt], 0x100000\n\t"
        "s_nop 3\n\t"
        "1:\n\t"
        "buffer_load_dwordx4 v[104:107], %[voff], %[rs], 0 offen sc1\n\t"
        "buffer_load_dwordx4 v[108:111], %[voff], %[rs], 0 offen offset:1024 sc1\n\t"
        "buffer_load_dwordx4 v[112:115], %[voff], %[rs], 0 offen offset:2048 sc1\n\t"
        "buffer_load_dwordx4 v[116:119], %[voff4], %[rs], 0 offen sc1\n\t"
        "s_waitcnt vmcnt(2)\n\t"
        "v_cmp_ne_u32_e32 vcc, %[tag], v105\n\t"
        "v_cmp_ne_u32_e64 %[t0], %[tag], v107\n\t"
        "v_cmp_ne_u32_e64 %[t1], %[tag], v109\n\t"
        "v_cmp_ne_u32_e64 %[t2], %[tag], v111\n\t"
        "s_or_b64 vcc, vcc, %[t0]\n\t"
        "s_or_b64 %[t1], %[t1], %[t2]\n\t"
        "s_or_b64 %[t0], vcc, %[t1]\n\t"
        "s_waitcnt vmcnt(0)\n\t"
        "v_cmp_ne_u32_e32 vcc, %[tag], v113\n\t"
        "v_cmp_ne_u32_e64 %[t1], %[tag], v115\n\t"
        "s_or_b64 %[t0], %[t0], vcc\n\t"
        "v_cmp_ne_u32_e32 vcc, %[tag], v117\n\t"
        "v_cmp_ne_u32_e64 %[t2], %[tag], v119\n\t"
        "s_or_b64 %[t0], %[t0], %[t1]\n\t"
        "s_or_b64 vcc, vcc, %[t2]\n\t"
        "s_or_b64 exec, vcc, %[t0]\n\t"
        "s_cbranch_execz 2f\n\t"
        "s_sub_u32 %[cnt], %[cnt], 1\n\t"
        "s_cbranch_scc0 1b\n\t"
        "2:\n\t"
        "s_mov_b64 exec, %[save]\n\t"
        "v_mov_b32 %[r0], v104\n\t"
        "v_mov_b32 %[r1], v106\n\t"
        "v_mov_b32 %[r2], v108\n\t"
        "v_mov_b32 %[r3], v110\n\t"
        "v_mov_b32 %[r4], v112\n\t"
        "v_mov_b32 %[r5], v114\n\t"
        "v_mov_b32 %[r6], v116\n\t"
        "v_mov_b32 %[r7], v118\n\t"
        : [r0] "=&v"(r[0]), [r1] "=&v"(r[1]), [r2] "=&v"(r[2]), [r3] "=&v"(r[3]), [r4] "=&v"(r[4]), [r5] "=&v"(r[5]), [r6] "=&v"(r[6]), [r7] "=&v"(r[7]),
          [save] "=&s"(save), [t0] "=&s"(t0), [t1] "=&s"(t1), [t2] "=&s"(t2), [cnt] "=&s"(cnt)
        : [voff] "v"(voff), [voff4] "v"(voff4), [rs] "s"(rs), [tag] "s"(tag)
        : "memory", "vcc", "scc", "v104", "v105", "v106", "v107", "v108", "v109", "v110", "v111", "v112", "v113", "v114", "v115", "v116", "v117", "v118", "v119");
}
#define P4_ISSUE(B) \
        "buffer_load_dwordx4 v[" #B "+0:" #B "+3], %[voff], %[rs], 0 offen sc1\n\t" \
        "buffer_load_dwordx4 v[" #B "+4:" #B "+7], %[voff], %[rs], 0 offen offset:1024 sc1\n\t" \
        "buffer_load_dwordx4 v[" #B "+8:" #B "+11], %[voff], %[rs], 0 offen offset:2048 sc1\n\t" \
        "buffer_load_dwordx4 v[" #B "+12:" #B "+15], %[voff4], %[rs], 0 offen sc1\n\t"
#define P4_CHECK(B) \
        "s_waitcnt vmcnt(4)\n\t" \
        "v_cmp_ne_u32_e32 vcc, %[tag], v[" #B "+1]\n\t" \
        "v_cmp_ne_u32_e64 %[t0], %[tag], v[" #B "+3]\n\t" \
        "v_cmp_ne_u32_e64 %[t1], %[tag], v[" #B "+5]\n\t" \
        "v_cmp_ne_u32_e64 %[t2], %[tag], v[" #B "+7]\n\t" \
        "s_or_b64 vcc, vcc, %[t0]\n\t" \
        "s_or_b64 %[t1], %[t1], %[t2]\n\t" \
        "s_or_b64 %[t0], vcc, %[t1]\n\t" \
        "v_cmp_ne_u32_e32 vcc, %[tag], v[" #B "+9]\n\t" \
        "v_cmp_ne_u32_e64 %[t1], %[tag], v[" #B "+11]\n\t" \
        "s_or_b64 %[t0], %[t0], vcc\n\t" \
        "v_cmp_ne_u32_e32 vcc, %[tag], v[" #B "+13]\n\t" \
        "v_cmp_ne_u32_e64 %[t2], %[tag], v[" #B "+15]\n\t" \
        "s_or_b64 %[t0], %[t0], %[t1]\n\t" \
        "s_or_b64 vcc, vcc, %[t2]\n\t" \
        "s_or_b64 %[t0], vcc, %[t0]\n\t" \
        "s_andn2_b64 exec, exec, %[t0]\n\t" \
        "v_mov_b32 %[r0], v[" #B "+0]\n\t" \
        "v_mov_b32 %[r1], v[" #B "+2]\n\t" \
        "v_mov_b32 %[r2], v[" #B "+4]\n\t" \
        "v_mov_b32 %[r3], v[" #B "+6]\n\t" \
        "v_mov_b32 %[r4], v[" #B "+8]\n\t" \
        "v_mov_b32 %[r5], v[" #B "+10]\n\t" \
        "v_mov_b32 %[r6], v[" #B "+12]\n\t" \
        "v_mov_b32 %[r7], v[" #B "+14]\n\t" \
        "s_mov_b64 exec, %[t0]\n\t" \
        "s_cmp_eq_u64 %[t0], 0\n\t" \
        "s_cbranch_scc1 2f\n\t"
__device__ __forceinline__ void poll4x2(__amdgpu_buffer_rsrc_t rs, int voff, int voff4, unsigned tag, float (&r)[8])
{
    unsigned long long save, t0, t1, t2; unsigned cnt;
    asm volatile(
        "s_mov_b64 %[save], exec\n\t"
        "s_mov_b32 %[cnt], 0x100000\n\t"
        "s_nop 3\n\t"
        P4_ISSUE(104)
        "1:\n\t"
        P4_SLEEP
        P4_ISSUE(120)
        P4_CHECK(104)
        P4_SLEEP
        P4_ISSUE(104)
        P4_CHECK(120)
        "s_sub_u32 %[cnt], %[cnt], 1\n\t"
        "s_cbranch_scc0 1b\n\t"
        "2:\n\t"
        "s_mov_b64 exec, %[save]\n\t"
        : [r0] "=&v"(r[0]), [r1] "=&v"(r[1]), [r2] "=&v"(r[2]), [r3] "=&v"(r[3]), [r4] "=&v"(r[4]), [r5] "=&v"(r[5]), [r6] "=&v"(r[6]), [r7] "=&v"(r[7]),
          [save] "=&s"(save), [t0] "=&s"(t0), [t1] "=&s"(t1), [t2] "=&s"(t2), [cnt] "=&s"(cnt)
        : [voff] "v"(voff), [voff4] "v"(voff4), [rs] "s"(rs), [tag] "s"(tag)
        : "memory", "vcc", "scc", "v104", "v105", "v106", "v107", "v108", "v109", "v110", "v111", "v112", "v113", "v114", "v115", "v116", "v117", "v118", "v119",
          "v120", "v121", "v122", "v123", "v124", "v125", "v126", "v127", "v128", "v129", "v130", "v131", "v132", "v133", "v134", "v135");
}
__device__ __forceinline__ void poll2(__amdgpu_buffer_rsrc_t rs, int voff, int voff2, unsigned tag, int sel, float& r0, float& r1, float& r2, float& r3)
{
    unsigned long long save, t0, t1, t2; unsigned cnt;
    POLL_BODY("s_cbranch_scc0");
}

#define DIAG_OFF (32u << 20)
typedef GAS unsigned long long gu64d;
#define DSTAMP(k) do { if (threadIdx.x == 0) { __hip_atomic_store((gu64d*)(ws + DIAG_OFF) + blockIdx.x * 8 + (k), (unsigned long long)__builtin_amdgcn_s_memrealtime(), RLX_AGENT); } } while (0)
__global__ __launch_bounds__(512, 2) void geglu_decode_kernel(
    const unsigned short* __restrict__ x, const unsigned short* __restrict__ wg, const unsigned short* __restrict__ wu,
    const unsigned short* __restrict__ wd, unsigned short* out, unsigned char* ws)
{
    STAMP_DECL
    STAMP(0);
    const unsigned long long dg0 = __builtin_amdgcn_s_memrealtime();
    __shared__ __attribute__((aligned(16))) float lds[2048];
    const int tid = threadIdx.x, l = tid & 63;
    const int w = __builtin_amdgcn_readfirstlane(tid >> 6);
    const int b = blockIdx.x;
    const int cb = b >> 3, kp = b & 7;

    const int mat = w >> 2, ww = w & 3;
    const __amdgpu_buffer_rsrc_t rs_gu = __builtin_amdgcn_make_buffer_rsrc((void*)(mat ? wu : wg), 0, DM * DF * 2, RSRC_W3);
    const __amdgpu_buffer_rsrc_t rs_d = __builtin_amdgcn_make_buffer_rsrc((void*)wd, 0, DM * DF * 2, RSRC_W3);
    const __amdgpu_buffer_rsrc_t rs_x = __builtin_amdgcn_make_buffer_rsrc((void*)x, 0, DM * 2, RSRC_W3);
    const __amdgpu_buffer_rsrc_t rs_ws = __builtin_amdgcn_make_buffer_rsrc((void*)ws, 0, WS_BYTES, RSRC_W3);
    const __amdgpu_buffer_rsrc_t rs_o = __builtin_amdgcn_make_buffer_rsrc((void*)out, 0, DM * 2, RSRC_W3);

    unsigned ep;
    asm volatile("s_load_dword %0, %1, 0x0" : "=s"(ep) : "s"(ws), "s"(x), "s"(wg), "s"(wu), "s"(wd), "s"(out) : "memory");
    const int q = l >> 4, n = l & 15;
    const bool late = kp >= 4;
    const int base = late ? 64 * NE + 16 * NL * (kp - 4) : 16 * NE * kp;
    const int row0 = base + 4 * ww + q;
    const int voff = row0 * (DF * 2) + (128 * cb + 8 * n) * 2;
    const int voff2 = late ? OOB : voff;
    v4u wv[NE];
    wv[0] = __builtin_amdgcn_raw_buffer_load_b128(rs_gu, voff, 0, AUX_NT);
    const unsigned xs = __builtin_amdgcn_raw_buffer_load_b16(rs_x, (n < (late ? NL : NE)) ? (row0 + 16 * n) * 2 : OOB, 0, 0);
#pragma unroll
    for (int i = 1; i < NE; ++i) wv[i] = __builtin_amdgcn_raw_buffer_load_b128(rs_gu, i < NL ? voff : voff2, i * 16 * (DF * 2), AUX_NT);
    const int doff = (128 * cb + 16 * w + 4 * q) * (DM * 2) + (128 * kp + 8 * n) * 2;
    v4u dv[4];
    const int pub1 = MB1_OFF + (8 * cb + (tid >> 4)) * 2048 + kp * 256 + (tid & 15) * 16;
    const int pub2 = MB2_OFF + (8 * kp + (l >> 3)) * RED_BYTES + cb * 128 + (l & 7) * 16;
    __builtin_amdgcn_sched_barrier(0);
    asm volatile("s_waitcnt lgkmcnt(0)" : "+s"(ep) :: "memory");
    const unsigned tag = ep * 1664525u + 1013904223u;
    if (w < 2) {
        const v4u z = {0u, ~tag, 0u, ~tag};
        __builtin_amdgcn_raw_buffer_store_b128(z, rs_ws, pub1, 0, AUX_SC1);
        if (PREWRITE > 1 && w == 0) __builtin_amdgcn_raw_buffer_store_b128(z, rs_ws, pub2, 0, AUX_SC1);
    }
    __builtin_amdgcn_sched_barrier(0);
    STAMP(1);
    if (threadIdx.x == 0) __hip_atomic_store((gu64d*)(ws + DIAG_OFF) + blockIdx.x * 8 + 0, dg0, RLX_AGENT);

    float acc[8];
#pragma unroll
    for (int k = 0; k < 8; ++k) acc[k] = 0.f;
    const int xf = (int)(xs << 16);
#define FMA_UNIT(i) if constexpr ((i) < NE) { \
        const float xi = __builtin_bit_cast(float, __builtin_amdgcn_update_dpp(0, xf, 0x150 + (i), 0xf, 0xf, false));   \
        _Pragma("unroll") for (int k = 0; k < 4; ++k) { \
            acc[2 * k] = fmaf(xi, bf_lo(wv[i][k]), acc[2 * k]); \
            acc[2 * k + 1] = fmaf(xi, bf_hi(wv[i][k]), acc[2 * k + 1]); } }
    FMA_UNIT(0) FMA_UNIT(1) FMA_UNIT(2) FMA_UNIT(3) FMA_UNIT(4) FMA_UNIT(5) FMA_UNIT(6) FMA_UNIT(7) FMA_UNIT(8) FMA_UNIT(9) FMA_UNIT(10) FMA_UNIT(11)
#undef FMA_UNIT
    asm volatile("" :: "v"(wv[NE - 1]));
    __builtin_amdgcn_sched_barrier(0);
#pragma unroll
    for (int k = 0; k < 4; ++k) dv[k] = __builtin_amdgcn_raw_buffer_load_b128(rs_d, doff, k * (DM * 2), AUX_NT);
    __builtin_amdgcn_sched_barrier(0);
    STAMP(2);
    {
        const float b0 = swap32_add(acc[0], acc[4]), b1 = swap32_add(acc[1], acc[5]);
        const float b2 = swap32_add(acc[2], acc[6]), b3 = swap32_add(acc[3], acc[7]);
        v2f c; c[0] = swap16_add(b0, b2); c[1] = swap16_add(b1, b3);
        *(v2f*)&lds[w * 128 + 8 * n + 2 * q] = c;
    }
    const unsigned msk = ((n & 3) < 2) ? ((n & 1) ? 0xffff0000u : 0x0000ffffu) : 0u;
    unsigned mk[4];
#pragma unroll
    for (int r = 0; r < 4; ++r) mk[r] = ((n >> 2) == r) ? msk : 0u;
    int off1 = MB1_OFF + (8 * cb + w) * 2048 + ((l >> 2) & 3) * 256 + (4 * q + (l & 3)) * 16, off1b = off1 + 1024;
    asm volatile("" : "+v"(mk[0]), "+v"(mk[1]), "+v"(mk[2]), "+v"(mk[3]), "+v"(off1), "+v"(off1b));
    __syncthreads();
    STAMP(3);
    DSTAMP(1);
    if (w < 2) {
        const float g = (lds[tid] + lds[128 + tid]) + (lds[256 + tid] + lds[384 + tid]);
        const float u = (lds[512 + tid] + lds[640 + tid]) + (lds[768 + tid] + lds[896 + tid]);
        const v4u p = {f2u(g), tag, f2u(u), tag};
        asm volatile("s_waitcnt vmcnt(4)" ::: "memory");
        __builtin_amdgcn_raw_buffer_store_b128(p, rs_ws, pub1, 0, AUX_SC1);
    }
    STAMP(4);
#define H1_WAIT 14
    if (H1_WAIT) __builtin_amdgcn_s_sleep(H1_WAIT);
    float p0, p1, p2, p3;
#define H1_SLP 0
    poll1<H1_SLP>(rs_ws, off1, off1b, tag, p0, p1, p2, p3);
    STAMP(5);
    DSTAMP(2);
    unsigned hpk;
    {
        float g = p0 + p2, u = p1 + p3;
        g = dpp_add<DPP_ROR(4)>(g); u = dpp_add<DPP_ROR(4)>(u);
        g = dpp_add<DPP_ROR(8)>(g); u = dpp_add<DPP_ROR(8)>(u);
        g = rbf(g); u = rbf(u);
        const float t = g * fmaf(g * g, -0.10294323970f, -2.30220819814f);
        const float ge = rbf(g * __builtin_amdgcn_rcpf(1.0f + __builtin_amdgcn_exp2f(t)));
        const unsigned hb = f2bf(ge * u);
        hpk = hb | (hb << 16);
    }
    v4f o = {0.f, 0.f, 0.f, 0.f};
#define STEP(i) { const unsigned hh = (unsigned)__builtin_amdgcn_update_dpp(0, (int)hpk, DPP_QUAD(i), 0xf, 0xf, true); \
        const v4u a = {hh & mk[0], hh & mk[1], hh & mk[2], hh & mk[3]}; \
        o = __builtin_amdgcn_mfma_f32_16x16x32_bf16(__builtin_bit_cast(v8s, a), __builtin_bit_cast(v8s, dv[i]), o, 0, 0, 0); }
    STEP(0) STEP(1) STEP(2) STEP(3)
#undef STEP
    {
        v2f c; c[0] = o[0]; c[1] = o[1];
        *(v2f*)&lds[1024 + w * 128 + 8 * n + 2 * q] = c;
    }
    __syncthreads();
    STAMP(6);
    if (w == 0) {
        DSTAMP(3);
        asm volatile("s_waitcnt vmcnt(0)" ::: "memory");
        v2f s[8];
#pragma unroll
        for (int k = 0; k < 8; ++k) s[k] = *(const v2f*)&lds[1024 + k * 128 + 2 * l];
        const v2f t = ((s[0] + s[1]) + (s[2] + s[3])) + ((s[4] + s[5]) + (s[6] + s[7]));
        const v4u p = {f2u(t[0]), tag, f2u(t[1]), tag};
        __builtin_amdgcn_raw_buffer_store_b128(p, rs_ws, pub2, 0, AUX_SC1);
    }
    STAMP(7);

    constexpr int FW = 0;
    if (b >= NRED || w != FW) { STAMP_FLUSH(); return; }
    {
        const int o0 = MB2_OFF + b * RED_BYTES + l * 16;
        float v[8];
        poll4x2(rs_ws, o0, l < 32 ? o0 + 3072 : o0, tag, v);
        STAMP(8);
        const float s0 = ((v[0] + v[2]) + v[4]) + (l < 32 ? v[6] : 0.f), s1 = ((v[1] + v[3]) + v[5]) + (l < 32 ? v[7] : 0.f);
        float s = swap32_add(s0, s1);
        s = swap16_add(s, s);
        s = dpp_add<DPP_ROR(8)>(s);
        if ((l & 24) == 0) __builtin_amdgcn_raw_buffer_store_b16((unsigned short)f2bf(s), rs_o, (16 * b + 2 * (l & 7) + (l >> 5)) * 2, 0, 0);
        if (b == 0 && l == 0) __hip_atomic_store((gu32*)ws, tag, RLX_AGENT);
    }

    if (b == 0) {
        unsigned long long t0 = ~0ull, mx = 0, acc = 0; int cnt = 0;
        const int K = 0, SEL = -1;
        for (int i = l; i < NWG; i += 64) {
            const unsigned long long s0 = __hip_atomic_load((gu64d*)(ws + DIAG_OFF) + i * 8 + 0, RLX_AGENT);
            const unsigned long long sk = __hip_atomic_load((gu64d*)(ws + DIAG_OFF) + i * 8 + K, RLX_AGENT);
            t0 = s0 < t0 ? s0 : t0;
            if (SEL < 0 || (i & 7) == SEL || (SEL == 8 && (i & 7) < 4) || (SEL == 9 && (i & 7) >= 4)) { mx = sk > mx ? sk : mx; acc += sk & 0xffffff; ++cnt; }
        }
        for (int m = 32; m >= 1; m >>= 1) { unsigned long long o = __shfl_xor(t0, m); t0 = o < t0 ? o : t0; o = __shfl_xor(mx, m); mx = o > mx ? o : mx; }
        const unsigned long long now = __builtin_amdgcn_s_memrealtime();
        const unsigned long long val = 0;
        const unsigned long long until = now + 4 * val;
        while (__builtin_amdgcn_s_memrealtime() < until) {}
    }
    STAMP(9);
    STAMP_FLUSH();
}

extern "C" void kernel_launch(void* const* d_in, const int* in_sizes, int n_in,
                              void* d_out, int out_size, void* d_ws, size_t ws_size,
                              hipStream_t stream) {
    geglu_decode_kernel<<<NWG, 512, 0, stream>>>((const unsigned short*)d_in[0], (const unsigned short*)d_in[1],
        (const unsigned short*)d_in[2], (const unsigned short*)d_in[3], (unsigned short*)d_out, (unsigned char*)d_ws);
}
```

```cpp
#include <hip/hip_runtime.h>
#include <hip/hip_bf16.h>
#include <stdint.h>

typedef unsigned v4u __attribute__((ext_vector_type(4)));
typedef float v4f __attribute__((ext_vector_type(4)));
typedef float v2f __attribute__((ext_vector_type(2)));
typedef short v8s __attribute__((ext_vector_type(8)));
#define GAS __attribute__((address_space(1)))
typedef GAS unsigned gu32;
#define RLX_AGENT __ATOMIC_RELAXED, __HIP_MEMORY_SCOPE_AGENT

constexpr int DM = 1024, DF = 3584;
constexpr int NWG = 224;
constexpr int NGRP = 28;
constexpr int NXCD = 8, PER_XCD = NWG / NXCD;
constexpr int NRED = 64;
constexpr int MB1_OFF = 4096;
constexpr int MB1_BYTES = NWG * 2048;
constexpr int MB2_OFF = MB1_OFF + MB1_BYTES;
constexpr int RED_BYTES = 4096;
constexpr int MB2_BYTES = NRED * RED_BYTES;
constexpr int WS_BYTES = MB2_OFF + MB2_BYTES;
constexpr int AUX_NT = 2, AUX_SC1 = 16;
constexpr int RSRC_W3 = 0x00020000;

__device__ __forceinline__ float u2f(unsigned v) { return __builtin_bit_cast(float, v); }
__device__ __forceinline__ unsigned f2u(float v) { return __builtin_bit_cast(unsigned, v); }
__device__ __forceinline__ float bf_lo(unsigned v) { return __builtin_bit_cast(float, v << 16); }
__device__ __forceinline__ float bf_hi(unsigned v) { return __builtin_bit_cast(float, v & 0xffff0000u); }
__device__ __forceinline__ unsigned f2bf(float f) {
    __hip_bfloat16 b = __float2bfloat16(f);
    return (unsigned)__builtin_bit_cast(unsigned short, b);
}
__device__ __forceinline__ float rbf(float f) { return __builtin_bit_cast(float, f2bf(f) << 16); }

template <int CTRL> __device__ __forceinline__ float dpp_add(float v) {
    int t = __builtin_amdgcn_update_dpp(0, __builtin_bit_cast(int, v), CTRL, 0xf, 0xf, true);
    return v + __builtin_bit_cast(float, t);
}
__device__ __forceinline__ float swap32_add(float a, float b) {
    auto r = __builtin_amdgcn_permlane32_swap(f2u(a), f2u(b), false, false);
    return u2f(r[0]) + u2f(r[1]);
}
__device__ __forceinline__ float swap16_add(float a, float b) {
    auto r = __builtin_amdgcn_permlane16_swap(f2u(a), f2u(b), false, false);
    return u2f(r[0]) + u2f(r[1]);
}
#define STAMP(k)
#define STAMP_DECL
#define STAMP_FLUSH()
constexpr int DPP_ROR(int n) { return 0x120 + n; }
constexpr int DPP_QUAD(int i) { return i | (i << 2) | (i << 4) | (i << 6); }
#define PREWARM 0
#define PREWRITE 2
#define P4_SLEEP "s_sleep 2\n\t"
#define H2_D2 1
#define NE 9
constexpr int NL = 16 - NE;
constexpr int OOB = 0x40000000;
#define WD_DELAY 8

#define POLL_ISSUE(R0, R1) \
    "buffer_load_dwordx4 " R0 ", %[voff], %[rs], 0 offen sc1\n\t" \
    "buffer_load_dwordx4 " R1 ", %[voff2], %[rs], 0 offen sc1\n\t"
#define POLL_CHECK(X0, Y0, Z0, W0, X1, Y1, Z1, W1) \
    "s_waitcnt vmcnt(4)\n\t" \
    "v_cmp_ne_u32_e32 vcc, %[tag], " Y0 "\n\t" \
    "v_cmp_ne_u32_e64 %[t0], %[tag], " W0 "\n\t" \
    "v_cmp_ne_u32_e64 %[t1], %[tag], " Y1 "\n\t" \
    "v_cmp_ne_u32_e64 %[t2], %[tag], " W1 "\n\t" \
    "s_or_b64 vcc, vcc, %[t0]\n\t" \
    "s_or_b64 %[t1], %[t1], %[t2]\n\t" \
    "s_or_b64 %[t0], vcc, %[t1]\n\t" \
    "s_andn2_b64 exec, exec, %[t0]\n\t" \
    "v_mov_b32 %[r0], " X0 "\n\t" \
    "v_mov_b32 %[r1], " Z0 "\n\t" \
    "v_mov_b32 %[r2], " X1 "\n\t" \
    "v_mov_b32 %[r3], " Z1 "\n\t" \
    "s_mov_b64 exec, %[t0]\n\t" \
    "s_cmp_eq_u64 %[t0], 0\n\t" \
    "s_cbranch_scc1 2f\n\t"
#define POLL_BODY(SKIP) \
    asm volatile( \
        "s_cmp_eq_u32 %[sel], 0\n\t" \
        SKIP " 3f\n\t" \
        "s_mov_b64 %[save], exec\n\t" \
        "s_mov_b32 %[cnt], 0x100000\n\t" \
        "s_nop 3\n\t" \
        POLL_ISSUE("v[104:107]", "v[108:111]") \
        POLL_ISSUE("v[112:115]", "v[116:119]") \
        "1:\n\t" \
        POLL_ISSUE("v[120:123]", "v[124:127]") \
        POLL_CHECK("v104", "v105", "v106", "v107", "v108", "v109", "v110", "v111") \
        POLL_ISSUE("v[104:107]", "v[108:111]") \
        POLL_CHECK("v112", "v113", "v114", "v115", "v116", "v117", "v118", "v119") \
        POLL_ISSUE("v[112:115]", "v[116:119]") \
        POLL_CHECK("v120", "v121", "v122", "v123", "v124", "v125", "v126", "v127") \
        "s_sub_u32 %[cnt], %[cnt], 1\n\t" \
        "s_cbranch_scc0 1b\n\t" \
        "2:\n\t" \
        "s_mov_b64 exec, %[save]\n\t" \
        "3:\n\t" \
        : [r0] "=&v"(r0), [r1] "=&v"(r1), [r2] "=&v"(r2), [r3] "=&v"(r3), [save] "=&s"(save), [t0] "=&s"(t0), [t1] "=&s"(t1), [t2] "=&s"(t2), [cnt] "=&s"(cnt) \
        : [voff] "v"(voff), [voff2] "v"(voff2), [rs] "s"(rs), [tag] "s"(tag), [sel] "s"(sel) \
        : "memory", "vcc", "scc", \
          "v104", "v105", "v106", "v107", "v108", "v109", "v110", "v111", "v112", "v113", "v114", "v115", \
          "v116", "v117", "v118", "v119", "v120", "v121", "v122", "v123", "v124", "v125", "v126", "v127")
#define POLL1_BODY(SLEEP) \
    asm volatile( \
        "s_mov_b64 %[save], exec\n\t" \
        "s_mov_b32 %[cnt], 0x100000\n\t" \
        "s_nop 3\n\t" \
        "1:\n\t" \
        POLL_ISSUE("v[104:107]", "v[108:111]") \
        "s_waitcnt vmcnt(0)\n\t" \
        "v_cmp_ne_u32_e32 vcc, %[tag], v105\n\t" \
        "v_cmp_ne_u32_e64 %[t0], %[tag], v107\n\t" \
        "v_cmp_ne_u32_e64 %[t1], %[tag], v109\n\t" \
        "v_cmp_ne_u32_e64 %[t2], %[tag], v111\n\t" \
        "s_or_b64 vcc, vcc, %[t0]\n\t" \
        "s_or_b64 %[t1], %[t1], %[t2]\n\t" \
        "s_or_b64 exec, vcc, %[t1]\n\t" \
        "s_cbranch_execz 2f\n\t" \
        SLEEP \
        "s_sub_u32 %[cnt], %[cnt], 1\n\t" \
        "s_cbranch_scc0 1b\n\t" \
        "2:\n\t" \
        "s_mov_b64 exec, %[save]\n\t" \
        "v_mov_b32 %[r0], v104\n\t" \
        "v_mov_b32 %[r1], v106\n\t" \
        "v_mov_b32 %[r2], v108\n\t" \
        "v_mov_b32 %[r3], v110\n\t" \
        : [r0] "=&v"(r0), [r1] "=&v"(r1), [r2] "=&v"(r2), [r3] "=&v"(r3), [save] "=&s"(save), [t0] "=&s"(t0), [t1] "=&s"(t1), [t2] "=&s"(t2), [cnt] "=&s"(cnt) \
        : [voff] "v"(voff), [voff2] "v"(voff2), [rs] "s"(rs), [tag] "s"(tag) \
        : "memory", "vcc", "scc", "v104", "v105", "v106", "v107", "v108", "v109", "v110", "v111")
template <int SLP>
__device__ __forceinline__ void poll1(__amdgpu_buffer_rsrc_t rs, int voff, int voff2, unsigned tag, float& r0, float& r1, float& r2, float& r3)
{
    unsigned long long save, t0, t1, t2; unsigned cnt;
    if constexpr (SLP == 0) { POLL1_BODY(""); } else if constexpr (SLP == 1) { POLL1_BODY("s_sleep 2\n\t"); } else { POLL1_BODY("s_sleep 6\n\t"); }
}
__device__ __forceinline__ void poll4(__amdgpu_buffer_rsrc_t rs, int voff, int voff4, unsigned tag, float (&r)[8])
{
    unsigned long long save, t0, t1, t2; unsigned cnt;
    asm volatile(
        "s_mov_b64 %[save], exec\n\t"
        "s_mov_b32 %[cnt], 0x100000\n\t"
        "s_nop 3\n\t"
        "1:\n\t"
        "buffer_load_dwordx4 v[104:107], %[voff], %[rs], 0 offen sc1\n\t"
        "buffer_load_dwordx4 v[108:111], %[voff], %[rs], 0 offen offset:1024 sc1\n\t"
        "buffer_load_dwordx4 v[112:115], %[voff], %[rs], 0 offen offset:2048 sc1\n\t"
        "buffer_load_dwordx4 v[116:119], %[voff4], %[rs], 0 offen sc1\n\t"
        "s_waitcnt vmcnt(2)\n\t"
        "v_cmp_ne_u32_e32 vcc, %[tag], v105\n\t"
        "v_cmp_ne_u32_e64 %[t0], %[tag], v107\n\t"
        "v_cmp_ne_u32_e64 %[t1], %[tag], v109\n\t"
        "v_cmp_ne_u32_e64 %[t2], %[tag], v111\n\t"
        "s_or_b64 vcc, vcc, %[t0]\n\t"
        "s_or_b64 %[t1], %[t1], %[t2]\n\t"
        "s_or_b64 %[t0], vcc, %[t1]\n\t"
        "s_waitcnt vmcnt(0)\n\t"
        "v_cmp_ne_u32_e32 vcc, %[tag], v113\n\t"
        "v_cmp_ne_u32_e64 %[t1], %[tag], v115\n\t"
        "s_or_b64 %[t0], %[t0], vcc\n\t"
        "v_cmp_ne_u32_e32 vcc, %[tag], v117\n\t"
        "v_cmp_ne_u32_e64 %[t2], %[tag], v119\n\t"
        "s_or_b64 %[t0], %[t0], %[t1]\n\t"
        "s_or_b64 vcc, vcc, %[t2]\n\t"
        "s_or_b64 exec, vcc, %[t0]\n\t"
        "s_cbranch_execz 2f\n\t"
        "s_sub_u32 %[cnt], %[cnt], 1\n\t"
        "s_cbranch_scc0 1b\n\t"
        "2:\n\t"
        "s_mov_b64 exec, %[save]\n\t"
        "v_mov_b32 %[r0], v104\n\t"
        "v_mov_b32 %[r1], v106\n\t"
        "v_mov_b32 %[r2], v108\n\t"
        "v_mov_b32 %[r3], v110\n\t"
        "v_mov_b32 %[r4], v112\n\t"
        "v_mov_b32 %[r5], v114\n\t"
        "v_mov_b32 %[r6], v116\n\t"
        "v_mov_b32 %[r7], v118\n\t"
        : [r0] "=&v"(r[0]), [r1] "=&v"(r[1]), [r2] "=&v"(r[2]), [r3] "=&v"(r[3]), [r4] "=&v"(r[4]), [r5] "=&v"(r[5]), [r6] "=&v"(r[6]), [r7] "=&v"(r[7]),
          [save] "=&s"(save), [t0] "=&s"(t0), [t1] "=&s"(t1), [t2] "=&s"(t2), [cnt] "=&s"(cnt)
        : [voff] "v"(voff), [voff4] "v"(voff4), [rs] "s"(rs), [tag] "s"(tag)
        : "memory", "vcc", "scc", "v104", "v105", "v106", "v107", "v108", "v109", "v110", "v111", "v112", "v113", "v114", "v115", "v116", "v117", "v118", "v119");
}
#define P4_ISSUE(B) \
        "buffer_load_dwordx4 v[" #B "+0:" #B "+3], %[voff], %[rs], 0 offen sc1\n\t" \
        "buffer_load_dwordx4 v[" #B "+4:" #B "+7], %[voff], %[rs], 0 offen offset:1024 sc1\n\t" \
        "buffer_load_dwordx4 v[" #B "+8:" #B "+11], %[voff], %[rs], 0 offen offset:2048 sc1\n\t" \
        "buffer_load_dwordx4 v[" #B "+12:" #B "+15], %[voff4], %[rs], 0 offen sc1\n\t"
#define P4_CHECK(B) \
        "s_waitcnt vmcnt(4)\n\t" \
        "v_cmp_ne_u32_e32 vcc, %[tag], v[" #B "+1]\n\t" \
        "v_cmp_ne_u32_e64 %[t0], %[tag], v[" #B "+3]\n\t" \
        "v_cmp_ne_u32_e64 %[t1], %[tag], v[" #B "+5]\n\t" \
        "v_cmp_ne_u32_e64 %[t2], %[tag], v[" #B "+7]\n\t" \
        "s_or_b64 vcc, vcc, %[t0]\n\t" \
        "s_or_b64 %[t1], %[t1], %[t2]\n\t" \
        "s_or_b64 %[t0], vcc, %[t1]\n\t" \
        "v_cmp_ne_u32_e32 vcc, %[tag], v[" #B "+9]\n\t" \
        "v_cmp_ne_u32_e64 %[t1], %[tag], v[" #B "+11]\n\t" \
        "s_or_b64 %[t0], %[t0], vcc\n\t" \
        "v_cmp_ne_u32_e32 vcc, %[tag], v[" #B "+13]\n\t" \
        "v_cmp_ne_u32_e64 %[t2], %[tag], v[" #B "+15]\n\t" \
        "s_or_b64 %[t0], %[t0], %[t1]\n\t" \
        "s_or_b64 vcc, vcc, %[t2]\n\t" \
        "s_or_b64 %[t0], vcc, %[t0]\n\t" \
        "s_andn2_b64 exec, exec, %[t0]\n\t" \
        "v_mov_b32 %[r0], v[" #B "+0]\n\t" \
        "v_mov_b32 %[r1], v[" #B "+2]\n\t" \
        "v_mov_b32 %[r2], v[" #B "+4]\n\t" \
        "v_mov_b32 %[r3], v[" #B "+6]\n\t" \
        "v_mov_b32 %[r4], v[" #B "+8]\n\t" \
        "v_mov_b32 %[r5], v[" #B "+10]\n\t" \
        "v_mov_b32 %[r6], v[" #B "+12]\n\t" \
        "v_mov_b32 %[r7], v[" #B "+14]\n\t" \
        "s_mov_b64 exec, %[t0]\n\t" \
        "s_cmp_eq_u64 %[t0], 0\n\t" \
        "s_cbranch_scc1 2f\n\t"
__device__ __forceinline__ void poll4x2(__amdgpu_buffer_rsrc_t rs, int voff, int voff4, unsigned tag, float (&r)[8])
{
    unsigned long long save, t0, t1, t2; unsigned cnt;
    asm volatile(
        "s_mov_b64 %[save], exec\n\t"
        "s_mov_b32 %[cnt], 0x100000\n\t"
        "s_nop 3\n\t"
        P4_ISSUE(104)
        "1:\n\t"
        P4_SLEEP
        P4_ISSUE(120)
        P4_CHECK(104)
        P4_SLEEP
        P4_ISSUE(104)
        P4_CHECK(120)
        "s_sub_u32 %[cnt], %[cnt], 1\n\t"
        "s_cbranch_scc0 1b\n\t"
        "2:\n\t"
        "s_mov_b64 exec, %[save]\n\t"
        : [r0] "=&v"(r[0]), [r1] "=&v"(r[1]), [r2] "=&v"(r[2]), [r3] "=&v"(r[3]), [r4] "=&v"(r[4]), [r5] "=&v"(r[5]), [r6] "=&v"(r[6]), [r7] "=&v"(r[7]),
          [save] "=&s"(save), [t0] "=&s"(t0), [t1] "=&s"(t1), [t2] "=&s"(t2), [cnt] "=&s"(cnt)
        : [voff] "v"(voff), [voff4] "v"(voff4), [rs] "s"(rs), [tag] "s"(tag)
        : "memory", "vcc", "scc", "v104", "v105", "v106", "v107", "v108", "v109", "v110", "v111", "v112", "v113", "v114", "v115", "v116", "v117", "v118", "v119",
          "v120", "v121", "v122", "v123", "v124", "v125", "v126", "v127", "v128", "v129", "v130", "v131", "v132", "v133", "v134", "v135");
}
__device__ __forceinline__ void poll2(__amdgpu_buffer_rsrc_t rs, int voff, int voff2, unsigned tag, int sel, float& r0, float& r1, float& r2, float& r3)
{
    unsigned long long save, t0, t1, t2; unsigned cnt;
    POLL_BODY("s_cbranch_scc0");
}

#define DIAG_OFF (32u << 20)
typedef GAS unsigned long long gu64d;
#define DSTAMP(k) do { if (threadIdx.x == 0) { __hip_atomic_store((gu64d*)(ws + DIAG_OFF) + blockIdx.x * 8 + (k), (unsigned long long)__builtin_amdgcn_s_memrealtime(), RLX_AGENT); } } while (0)
__global__ __launch_bounds__(512, 2) void geglu_decode_kernel(
    const unsigned short* __restrict__ x, const unsigned short* __restrict__ wg, const unsigned short* __restrict__ wu,
    const unsigned short* __restrict__ wd, unsigned short* out, unsigned char* ws)
{
    STAMP_DECL
    STAMP(0);
    const unsigned long long dg0 = __builtin_amdgcn_s_memrealtime();
    __shared__ __attribute__((aligned(16))) float lds[2048];
    const int tid = threadIdx.x, l = tid & 63;
    const int w = __builtin_amdgcn_readfirstlane(tid >> 6);
    const int b = blockIdx.x;
    const int cb = b >> 3, kp = b & 7;

    const int mat = w >> 2, ww = w & 3;
    const __amdgpu_buffer_rsrc_t rs_gu = __builtin_amdgcn_make_buffer_rsrc((void*)(mat ? wu : wg), 0, DM * DF * 2, RSRC_W3);
    const __amdgpu_buffer_rsrc_t rs_d = __builtin_amdgcn_make_buffer_rsrc((void*)wd, 0, DM * DF * 2, RSRC_W3);
    const __amdgpu_buffer_rsrc_t rs_x = __builtin_amdgcn_make_buffer_rsrc((void*)x, 0, DM * 2, RSRC_W3);
    const __amdgpu_buffer_rsrc_t rs_ws = __builtin_amdgcn_make_buffer_rsrc((void*)ws, 0, WS_BYTES, RSRC_W3);
    const __amdgpu_buffer_rsrc_t rs_o = __builtin_amdgcn_make_buffer_rsrc((void*)out, 0, DM * 2, RSRC_W3);

    unsigned ep;
    asm volatile("s_load_dword %0, %1, 0x0" : "=s"(ep) : "s"(ws), "s"(x), "s"(wg), "s"(wu), "s"(wd), "s"(out) : "memory");
    const int q = l >> 4, n = l & 15;
    const bool late = kp >= 4;
    const int base = late ? 64 * NE + 16 * NL * (kp - 4) : 16 * NE * kp;
    const int row0 = base + 4 * ww + q;
    const int voff = row0 * (DF * 2) + (128 * cb + 8 * n) * 2;
    const int voff2 = late ? OOB : voff;
    v4u wv[NE];
    wv[0] = __builtin_amdgcn_raw_buffer_load_b128(rs_gu, voff, 0, AUX_NT);
    const unsigned xs = __builtin_amdgcn_raw_buffer_load_b16(rs_x, (n < (late ? NL : NE)) ? (row0 + 16 * n) * 2 : OOB, 0, 0);
#pragma unroll
    for (int i = 1; i < NE; ++i) wv[i] = __builtin_amdgcn_raw_buffer_load_b128(rs_gu, i < NL ? voff : voff2, i * 16 * (DF * 2), AUX_NT);
    const int doff = (128 * cb + 16 * w + 4 * q) * (DM * 2) + (128 * kp + 8 * n) * 2;
    v4u dv[4];
    const int pub1 = MB1_OFF + (8 * cb + (tid >> 4)) * 2048 + kp * 256 + (tid & 15) * 16;
    const int pub2 = MB2_OFF + (8 * kp + (l >> 3)) * RED_BYTES + cb * 128 + (l & 7) * 16;
    __builtin_amdgcn_sched_barrier(0);
    asm volatile("s_waitcnt lgkmcnt(0)" : "+s"(ep) :: "memory");
    const unsigned tag = ep * 1664525u + 1013904223u;
    if (w < 2) {
        const v4u z = {0u, ~tag, 0u, ~tag};
        __builtin_amdgcn_raw_buffer_store_b128(z, rs_ws, pub1, 0, AUX_SC1);
        if (PREWRITE > 1 && w == 0) __builtin_amdgcn_raw_buffer_store_b128(z, rs_ws, pub2, 0, AUX_SC1);
    }
    __builtin_amdgcn_sched_barrier(0);
    STAMP(1);
    if (threadIdx.x == 0) __hip_atomic_store((gu64d*)(ws + DIAG_OFF) + blockIdx.x * 8 + 0, dg0, RLX_AGENT);

    float acc[8];
#pragma unroll
    for (int k = 0; k < 8; ++k) acc[k] = 0.f;
    const int xf = (int)(xs << 16);
#define FMA_UNIT(i) if constexpr ((i) < NE) { \
        const float xi = __builtin_bit_cast(float, __builtin_amdgcn_update_dpp(0, xf, 0x150 + (i), 0xf, 0xf, false));   \
        _Pragma("unroll") for (int k = 0; k < 4; ++k) { \
            acc[2 * k] = fmaf(xi, bf_lo(wv[i][k]), acc[2 * k]); \
            acc[2 * k + 1] = fmaf(xi, bf_hi(wv[i][k]), acc[2 * k + 1]); } }
    FMA_UNIT(0) FMA_UNIT(1) FMA_UNIT(2) FMA_UNIT(3) FMA_UNIT(4) FMA_UNIT(5) FMA_UNIT(6) FMA_UNIT(7) FMA_UNIT(8) FMA_UNIT(9) FMA_UNIT(10) FMA_UNIT(11)
#undef FMA_UNIT
    asm volatile("" :: "v"(wv[NE - 1]));
    __builtin_amdgcn_sched_barrier(0);
#pragma unroll
    for (int k = 0; k < 4; ++k) dv[k] = __builtin_amdgcn_raw_buffer_load_b128(rs_d, doff, k * (DM * 2), AUX_NT);
    __builtin_amdgcn_sched_barrier(0);
    STAMP(2);
    {
        const float b0 = swap32_add(acc[0], acc[4]), b1 = swap32_add(acc[1], acc[5]);
        const float b2 = swap32_add(acc[2], acc[6]), b3 = swap32_add(acc[3], acc[7]);
        v2f c; c[0] = swap16_add(b0, b2); c[1] = swap16_add(b1, b3);
        *(v2f*)&lds[w * 128 + 8 * n + 2 * q] = c;
    }
    const unsigned msk = ((n & 3) < 2) ? ((n & 1) ? 0xffff0000u : 0x0000ffffu) : 0u;
    unsigned mk[4];
#pragma unroll
    for (int r = 0; r < 4; ++r) mk[r] = ((n >> 2) == r) ? msk : 0u;
    int off1 = MB1_OFF + (8 * cb + w) * 2048 + ((l >> 2) & 3) * 256 + (4 * q + (l & 3)) * 16, off1b = off1 + 1024;
    asm volatile("" : "+v"(mk[0]), "+v"(mk[1]), "+v"(mk[2]), "+v"(mk[3]), "+v"(off1), "+v"(off1b));
    __syncthreads();
    STAMP(3);
    DSTAMP(1);
    if (w < 2) {
        const float g = (lds[tid] + lds[128 + tid]) + (lds[256 + tid] + lds[384 + tid]);
        const float u = (lds[512 + tid] + lds[640 + tid]) + (lds[768 + tid] + lds[896 + tid]);
        const v4u p = {f2u(g), tag, f2u(u), tag};
        asm volatile("s_waitcnt vmcnt(4)" ::: "memory");
        __builtin_amdgcn_raw_buffer_store_b128(p, rs_ws, pub1, 0, AUX_SC1);
    }
    STAMP(4);
#define H1_WAIT 14
    if (H1_WAIT) __builtin_amdgcn_s_sleep(H1_WAIT);
    float p0, p1, p2, p3;
#define H1_SLP 0
    poll1<H1_SLP>(rs_ws, off1, off1b, tag, p0, p1, p2, p3);
    STAMP(5);
    DSTAMP(2);
    unsigned hpk;
    {
        float g = p0 + p2, u = p1 + p3;
        g = dpp_add<DPP_ROR(4)>(g); u = dpp_add<DPP_ROR(4)>(u);
        g = dpp_add<DPP_ROR(8)>(g); u = dpp_add<DPP_ROR(8)>(u);
        g = rbf(g); u = rbf(u);
        const float t = g * fmaf(g * g, -0.10294323970f, -2.30220819814f);
        const float ge = rbf(g * __builtin_amdgcn_rcpf(1.0f + __builtin_amdgcn_exp2f(t)));
        const unsigned hb = f2bf(ge * u);
        hpk = hb | (hb << 16);
    }
    v4f o = {0.f, 0.f, 0.f, 0.f};
#define STEP(i) { const unsigned hh = (unsigned)__builtin_amdgcn_update_dpp(0, (int)hpk, DPP_QUAD(i), 0xf, 0xf, true); \
        const v4u a = {hh & mk[0], hh & mk[1], hh & mk[2], hh & mk[3]}; \
        o = __builtin_amdgcn_mfma_f32_16x16x32_bf16(__builtin_bit_cast(v8s, a), __builtin_bit_cast(v8s, dv[i]), o, 0, 0, 0); }
    STEP(0) STEP(1) STEP(2) STEP(3)
#undef STEP
    {
        v2f c; c[0] = o[0]; c[1] = o[1];
        *(v2f*)&lds[1024 + w * 128 + 8 * n + 2 * q] = c;
    }
    __syncthreads();
    STAMP(6);
    if (w == 0) {
        DSTAMP(3);
        asm volatile("s_waitcnt vmcnt(0)" ::: "memory");
        v2f s[8];
#pragma unroll
        for (int k = 0; k < 8; ++k) s[k] = *(const v2f*)&lds[1024 + k * 128 + 2 * l];
        const v2f t = ((s[0] + s[1]) + (s[2] + s[3])) + ((s[4] + s[5]) + (s[6] + s[7]));
        const v4u p = {f2u(t[0]), tag, f2u(t[1]), tag};
        __builtin_amdgcn_raw_buffer_store_b128(p, rs_ws, pub2, 0, AUX_SC1);
    }
    STAMP(7);

    constexpr int FW = 0;
    if (b >= NRED || w != FW) { STAMP_FLUSH(); return; }
    {
        const int o0 = MB2_OFF + b * RED_BYTES + l * 16;
        float v[8];
        poll4x2(rs_ws, o0, l < 32 ? o0 + 3072 : o0, tag, v);
        STAMP(8);
        const float s0 = ((v[0] + v[2]) + v[4]) + (l < 32 ? v[6] : 0.f), s1 = ((v[1] + v[3]) + v[5]) + (l < 32 ? v[7] : 0.f);
        float s = swap32_add(s0, s1);
        s = swap16_add(s, s);
        s = dpp_add<DPP_ROR(8)>(s);
        if ((l & 24) == 0) __builtin_amdgcn_raw_buffer_store_b16((unsigned short)f2bf(s), rs_o, (16 * b + 2 * (l & 7) + (l >> 5)) * 2, 0, 0);
        if (b == 0 && l == 0) __hip_atomic_store((gu32*)ws, tag, RLX_AGENT);
    }

    if (b == 0) {
        unsigned long long t0 = ~0ull, mx = 0, mn = ~0ull, acc = 0; unsigned cnt = 0;
        const int K = 0, SEL = -1;
        for (int i = l; i < NWG; i += 64) {
            const unsigned long long s0 = __hip_atomic_load((gu64d*)(ws + DIAG_OFF) + i * 8 + 0, RLX_AGENT);
            const unsigned long long sk = __hip_atomic_load((gu64d*)(ws + DIAG_OFF) + i * 8 + K, RLX_AGENT);
            t0 = s0 < t0 ? s0 : t0;
            if (SEL < 0 || (i & 7) == SEL || (SEL == 8 && (i & 7) < 4) || (SEL == 9 && (i & 7) >= 4)) { mx = sk > mx ? sk : mx; mn = sk < mn ? sk : mn; acc += sk; ++cnt; }
        }
        for (int m = 32; m >= 1; m >>= 1) { unsigned long long o = __shfl_xor(t0, m); t0 = o < t0 ? o : t0; o = __shfl_xor(mx, m); mx = o > mx ? o : mx; o = __shfl_xor(mn, m); mn = o < mn ? o : mn; acc += __shfl_xor(acc, m); cnt += __shfl_xor(cnt, m); }
        const unsigned long long mean = acc / cnt;
        const unsigned long long now = __builtin_amdgcn_s_memrealtime();
        const unsigned long long val = 0;
        const unsigned long long until = now + 4 * val;
        while (__builtin_amdgcn_s_memrealtime() < until) {}
    }
    STAMP(9);
    STAMP_FLUSH();
}

extern "C" void kernel_launch(void* const* d_in, const int* in_sizes, int n_in,
                              void* d_out, int out_size, void* d_ws, size_t ws_size,
                              hipStream_t stream) {
    geglu_decode_kernel<<<NWG, 512, 0, stream>>>((const unsigned short*)d_in[0], (const unsigned short*)d_in[1],
        (const unsigned short*)d_in[2], (const unsigned short*)d_in[3], (unsigned short*)d_out, (unsigned char*)d_ws);
}
```

```cpp
#include <hip/hip_runtime.h>
#include <hip/hip_bf16.h>
#include <stdint.h>

typedef unsigned v4u __attribute__((ext_vector_type(4)));
typedef float v4f __attribute__((ext_vector_type(4)));
typedef float v2f __attribute__((ext_vector_type(2)));
typedef short v8s __attribute__((ext_vector_type(8)));
#define GAS __attribute__((address_space(1)))
typedef GAS unsigned gu32;
#define RLX_AGENT __ATOMIC_RELAXED, __HIP_MEMORY_SCOPE_AGENT

constexpr int DM = 1024, DF = 3584;
constexpr int NWG = 224;
constexpr int NGRP = 28;
constexpr int NXCD = 8, PER_XCD = NWG / NXCD;
constexpr int NRED = 64;
constexpr int MB1_OFF = 4096;
constexpr int MB1_BYTES = NWG * 2048;
constexpr int MB2_OFF = MB1_OFF + MB1_BYTES;
constexpr int RED_BYTES = 4096;
constexpr int MB2_BYTES = NRED * RED_BYTES;
constexpr int WS_BYTES = MB2_OFF + MB2_BYTES;
constexpr int AUX_NT = 2, AUX_SC1 = 16;
constexpr int RSRC_W3 = 0x00020000;

__device__ __forceinline__ float u2f(unsigned v) { return __builtin_bit_cast(float, v); }
__device__ __forceinline__ unsigned f2u(float v) { return __builtin_bit_cast(unsigned, v); }
__device__ __forceinline__ float bf_lo(unsigned v) { return __builtin_bit_cast(float, v << 16); }
__device__ __forceinline__ float bf_hi(unsigned v) { return __builtin_bit_cast(float, v & 0xffff0000u); }
__device__ __forceinline__ unsigned f2bf(float f) {
    __hip_bfloat16 b = __float2bfloat16(f);
    return (unsigned)__builtin_bit_cast(unsigned short, b);
}
__device__ __forceinline__ float rbf(float f) { return __builtin_bit_cast(float, f2bf(f) << 16); }

template <int CTRL> __device__ __forceinline__ float dpp_add(float v) {
    int t = __builtin_amdgcn_update_dpp(0, __builtin_bit_cast(int, v), CTRL, 0xf, 0xf, true);
    return v + __builtin_bit_cast(float, t);
}
__device__ __forceinline__ float swap32_add(float a, float b) {
    auto r = __builtin_amdgcn_permlane32_swap(f2u(a), f2u(b), false, false);
    return u2f(r[0]) + u2f(r[1]);
}
__device__ __forceinline__ float swap16_add(float a, float b) {
    auto r = __builtin_amdgcn_permlane16_swap(f2u(a), f2u(b), false, false);
    return u2f(r[0]) + u2f(r[1]);
}
#define STAMP(k)
#define STAMP_DECL
#define STAMP_FLUSH()
constexpr int DPP_ROR(int n) { return 0x120 + n; }
constexpr int DPP_QUAD(int i) { return i | (i << 2) | (i << 4) | (i << 6); }
#define PREWARM 0
#define PREWRITE 2
#define P4_SLEEP "s_sleep 2\n\t"
#define H2_D2 1
#define UNITS 9, 9, 9, 9, 7, 7, 7, 7
constexpr int UN[8] = {UNITS};
constexpr int umax() { int m = 0; for (int i = 0; i < 8; ++i) m = UN[i] > m ? UN[i] : m; return m; }
constexpr int umin() { int m = 99; for (int i = 0; i < 8; ++i) m = UN[i] < m ? UN[i] : m; return m; }
constexpr unsigned upack() { unsigned p = 0; for (int i = 0; i < 8; ++i) p |= (unsigned)UN[i] << (4 * i); return p; }
constexpr unsigned long long bpack() { unsigned long long p = 0; int b = 0; for (int i = 0; i < 8; ++i) { p |= (unsigned long long)b << (8 * i); b += UN[i]; } return p; }
constexpr int usum() { int b = 0; for (int i = 0; i < 8; ++i) b += UN[i]; return b; }
static_assert(usum() == 64, "the units must cover the 1024 rows");
constexpr int NE = umax(), UMIN = umin();
constexpr unsigned U_PACK = upack();
constexpr unsigned long long B_PACK = bpack();
constexpr int OOB = 0x40000000;
#define WD_DELAY 8

#define POLL_ISSUE(R0, R1) \
    "buffer_load_dwordx4 " R0 ", %[voff], %[rs], 0 offen sc1\n\t" \
    "buffer_load_dwordx4 " R1 ", %[voff2], %[rs], 0 offen sc1\n\t"
#define POLL_CHECK(X0, Y0, Z0, W0, X1, Y1, Z1, W1) \
    "s_waitcnt vmcnt(4)\n\t" \
    "v_cmp_ne_u32_e32 vcc, %[tag], " Y0 "\n\t" \
    "v_cmp_ne_u32_e64 %[t0], %[tag], " W0 "\n\t" \
    "v_cmp_ne_u32_e64 %[t1], %[tag], " Y1 "\n\t" \
    "v_cmp_ne_u32_e64 %[t2], %[tag], " W1 "\n\t" \
    "s_or_b64 vcc, vcc, %[t0]\n\t" \
    "s_or_b64 %[t1], %[t1], %[t2]\n\t" \
    "s_or_b64 %[t0], vcc, %[t1]\n\t" \
    "s_andn2_b64 exec, exec, %[t0]\n\t" \
    "v_mov_b32 %[r0], " X0 "\n\t" \
    "v_mov_b32 %[r1], " Z0 "\n\t" \
    "v_mov_b32 %[r2], " X1 "\n\t" \
    "v_mov_b32 %[r3], " Z1 "\n\t" \
    "s_mov_b64 exec, %[t0]\n\t" \
    "s_cmp_eq_u64 %[t0], 0\n\t" \
    "s_cbranch_scc1 2f\n\t"
#define POLL_BODY(SKIP) \
    asm volatile( \
        "s_cmp_eq_u32 %[sel], 0\n\t" \
        SKIP " 3f\n\t" \
        "s_mov_b64 %[save], exec\n\t" \
        "s_mov_b32 %[cnt], 0x100000\n\t" \
        "s_nop 3\n\t" \
        POLL_ISSUE("v[104:107]", "v[108:111]") \
        POLL_ISSUE("v[112:115]", "v[116:119]") \
        "1:\n\t" \
        POLL_ISSUE("v[120:123]", "v[124:127]") \
        POLL_CHECK("v104", "v105", "v106", "v107", "v108", "v109", "v110", "v111") \
        POLL_ISSUE("v[104:107]", "v[108:111]") \
        POLL_CHECK("v112", "v113", "v114", "v115", "v116", "v117", "v118", "v119") \
        POLL_ISSUE("v[112:115]", "v[116:119]") \
        POLL_CHECK("v120", "v121", "v122", "v123", "v124", "v125", "v126", "v127") \
        "s_sub_u32 %[cnt], %[cnt], 1\n\t" \
        "s_cbranch_scc0 1b\n\t" \
        "2:\n\t" \
        "s_mov_b64 exec, %[save]\n\t" \
        "3:\n\t" \
        : [r0] "=&v"(r0), [r1] "=&v"(r1), [r2] "=&v"(r2), [r3] "=&v"(r3), [save] "=&s"(save), [t0] "=&s"(t0), [t1] "=&s"(t1), [t2] "=&s"(t2), [cnt] "=&s"(cnt) \
        : [voff] "v"(voff), [voff2] "v"(voff2), [rs] "s"(rs), [tag] "s"(tag), [sel] "s"(sel) \
        : "memory", "vcc", "scc", \
          "v104", "v105", "v106", "v107", "v108", "v109", "v110", "v111", "v112", "v113", "v114", "v115", \
          "v116", "v117", "v118", "v119", "v120", "v121", "v122", "v123", "v124", "v125", "v126", "v127")
#define POLL1_BODY(SLEEP) \
    asm volatile( \
        "s_mov_b64 %[save], exec\n\t" \
        "s_mov_b32 %[cnt], 0x100000\n\t" \
        "s_nop 3\n\t" \
        "1:\n\t" \
        POLL_ISSUE("v[104:107]", "v[108:111]") \
        "s_waitcnt vmcnt(0)\n\t" \
        "v_cmp_ne_u32_e32 vcc, %[tag], v105\n\t" \
        "v_cmp_ne_u32_e64 %[t0], %[tag], v107\n\t" \
        "v_cmp_ne_u32_e64 %[t1], %[tag], v109\n\t" \
        "v_cmp_ne_u32_e64 %[t2], %[tag], v111\n\t" \
        "s_or_b64 vcc, vcc, %[t0]\n\t" \
        "s_or_b64 %[t1], %[t1], %[t2]\n\t" \
        "s_or_b64 exec, vcc, %[t1]\n\t" \
        "s_cbranch_execz 2f\n\t" \
        SLEEP \
        "s_sub_u32 %[cnt], %[cnt], 1\n\t" \
        "s_cbranch_scc0 1b\n\t" \
        "2:\n\t" \
        "s_mov_b64 exec, %[save]\n\t" \
        "v_mov_b32 %[r0], v104\n\t" \
        "v_mov_b32 %[r1], v106\n\t" \
        "v_mov_b32 %[r2], v108\n\t" \
        "v_mov_b32 %[r3], v110\n\t" \
        : [r0] "=&v"(r0), [r1] "=&v"(r1), [r2] "=&v"(r2), [r3] "=&v"(r3), [save] "=&s"(save), [t0] "=&s"(t0), [t1] "=&s"(t1), [t2] "=&s"(t2), [cnt] "=&s"(cnt) \
        : [voff] "v"(voff), [voff2] "v"(voff2), [rs] "s"(rs), [tag] "s"(tag) \
        : "memory", "vcc", "scc", "v104", "v105", "v106", "v107", "v108", "v109", "v110", "v111")
template <int SLP>
__device__ __forceinline__ void poll1(__amdgpu_buffer_rsrc_t rs, int voff, int voff2, unsigned tag, float& r0, float& r1, float& r2, float& r3)
{
    unsigned long long save, t0, t1, t2; unsigned cnt;
    if constexpr (SLP == 0) { POLL1_BODY(""); } else if constexpr (SLP == 1) { POLL1_BODY("s_sleep 2\n\t"); } else { POLL1_BODY("s_sleep 6\n\t"); }
}
__device__ __forceinline__ void poll4(__amdgpu_buffer_rsrc_t rs, int voff, int voff4, unsigned tag, float (&r)[8])
{
    unsigned long long save, t0, t1, t2; unsigned cnt;
    asm volatile(
        "s_mov_b64 %[save], exec\n\t"
        "s_mov_b32 %[cnt], 0x100000\n\t"
        "s_nop 3\n\t"
        "1:\n\t"
        "buffer_load_dwordx4 v[104:107], %[voff], %[rs], 0 offen sc1\n\t"
        "buffer_load_dwordx4 v[108:111], %[voff], %[rs], 0 offen offset:1024 sc1\n\t"
        "buffer_load_dwordx4 v[112:115], %[voff], %[rs], 0 offen offset:2048 sc1\n\t"
        "buffer_load_dwordx4 v[116:119], %[voff4], %[rs], 0 offen sc1\n\t"
        "s_waitcnt vmcnt(2)\n\t"
        "v_cmp_ne_u32_e32 vcc, %[tag], v105\n\t"
        "v_cmp_ne_u32_e64 %[t0], %[tag], v107\n\t"
        "v_cmp_ne_u32_e64 %[t1], %[tag], v109\n\t"
        "v_cmp_ne_u32_e64 %[t2], %[tag], v111\n\t"
        "s_or_b64 vcc, vcc, %[t0]\n\t"
        "s_or_b64 %[t1], %[t1], %[t2]\n\t"
        "s_or_b64 %[t0], vcc, %[t1]\n\t"
        "s_waitcnt vmcnt(0)\n\t"
        "v_cmp_ne_u32_e32 vcc, %[tag], v113\n\t"
        "v_cmp_ne_u32_e64 %[t1], %[tag], v115\n\t"
        "s_or_b64 %[t0], %[t0], vcc\n\t"
        "v_cmp_ne_u32_e32 vcc, %[tag], v117\n\t"
        "v_cmp_ne_u32_e64 %[t2], %[tag], v119\n\t"
        "s_or_b64 %[t0], %[t0], %[t1]\n\t"
        "s_or_b64 vcc, vcc, %[t2]\n\t"
        "s_or_b64 exec, vcc, %[t0]\n\t"
        "s_cbranch_execz 2f\n\t"
        "s_sub_u32 %[cnt], %[cnt], 1\n\t"
        "s_cbranch_scc0 1b\n\t"
        "2:\n\t"
        "s_mov_b64 exec, %[save]\n\t"
        "v_mov_b32 %[r0], v104\n\t"
        "v_mov_b32 %[r1], v106\n\t"
        "v_mov_b32 %[r2], v108\n\t"
        "v_mov_b32 %[r3], v110\n\t"
        "v_mov_b32 %[r4], v112\n\t"
        "v_mov_b32 %[r5], v114\n\t"
        "v_mov_b32 %[r6], v116\n\t"
        "v_mov_b32 %[r7], v118\n\t"
        : [r0] "=&v"(r[0]), [r1] "=&v"(r[1]), [r2] "=&v"(r[2]), [r3] "=&v"(r[3]), [r4] "=&v"(r[4]), [r5] "=&v"(r[5]), [r6] "=&v"(r[6]), [r7] "=&v"(r[7]),
          [save] "=&s"(save), [t0] "=&s"(t0), [t1] "=&s"(t1), [t2] "=&s"(t2), [cnt] "=&s"(cnt)
        : [voff] "v"(voff), [voff4] "v"(voff4), [rs] "s"(rs), [tag] "s"(tag)
        : "memory", "vcc", "scc", "v104", "v105", "v106", "v107", "v108", "v109", "v110", "v111", "v112", "v113", "v114", "v115", "v116", "v117", "v118", "v119");
}
#define P4_ISSUE(B) \
        "buffer_load_dwordx4 v[" #B "+0:" #B "+3], %[voff], %[rs], 0 offen sc1\n\t" \
        "buffer_load_dwordx4 v[" #B "+4:" #B "+7], %[voff], %[rs], 0 offen offset:1024 sc1\n\t" \
        "buffer_load_dwordx4 v[" #B "+8:" #B "+11], %[voff], %[rs], 0 offen offset:2048 sc1\n\t" \
        "buffer_load_dwordx4 v[" #B "+12:" #B "+15], %[voff4], %[rs], 0 offen sc1\n\t"
#define P4_CHECK(B) \
        "s_waitcnt vmcnt(4)\n\t" \
        "v_cmp_ne_u32_e32 vcc, %[tag], v[" #B "+1]\n\t" \
        "v_cmp_ne_u32_e64 %[t0], %[tag], v[" #B "+3]\n\t" \
        "v_cmp_ne_u32_e64 %[t1], %[tag], v[" #B "+5]\n\t" \
        "v_cmp_ne_u32_e64 %[t2], %[tag], v[" #B "+7]\n\t" \
        "s_or_b64 vcc, vcc, %[t0]\n\t" \
        "s_or_b64 %[t1], %[t1], %[t2]\n\t" \
        "s_or_b64 %[t0], vcc, %[t1]\n\t" \
        "v_cmp_ne_u32_e32 vcc, %[tag], v[" #B "+9]\n\t" \
        "v_cmp_ne_u32_e64 %[t1], %[tag], v[" #B "+11]\n\t" \
        "s_or_b64 %[t0], %[t0], vcc\n\t" \
        "v_cmp_ne_u32_e32 vcc, %[tag], v[" #B "+13]\n\t" \
        "v_cmp_ne_u32_e64 %[t2], %[tag], v[" #B "+15]\n\t" \
        "s_or_b64 %[t0], %[t0], %[t1]\n\t" \
        "s_or_b64 vcc, vcc, %[t2]\n\t" \
        "s_or_b64 %[t0], vcc, %[t0]\n\t" \
        "s_andn2_b64 exec, exec, %[t0]\n\t" \
        "v_mov_b32 %[r0], v[" #B "+0]\n\t" \
        "v_mov_b32 %[r1], v[" #B "+2]\n\t" \
        "v_mov_b32 %[r2], v[" #B "+4]\n\t" \
        "v_mov_b32 %[r3], v[" #B "+6]\n\t" \
        "v_mov_b32 %[r4], v[" #B "+8]\n\t" \
        "v_mov_b32 %[r5], v[" #B "+10]\n\t" \
        "v_mov_b32 %[r6], v[" #B "+12]\n\t" \
        "v_mov_b32 %[r7], v[" #B "+14]\n\t" \
        "s_mov_b64 exec, %[t0]\n\t" \
        "s_cmp_eq_u64 %[t0], 0\n\t" \
        "s_cbranch_scc1 2f\n\t"
__device__ __forceinline__ void poll4x2(__amdgpu_buffer_rsrc_t rs, int voff, int voff4, unsigned tag, float (&r)[8])
{
    unsigned long long save, t0, t1, t2; unsigned cnt;
    asm volatile(
        "s_mov_b64 %[save], exec\n\t"
        "s_mov_b32 %[cnt], 0x100000\n\t"
        "s_nop 3\n\t"
        P4_ISSUE(104)
        "1:\n\t"
        P4_SLEEP
        P4_ISSUE(120)
        P4_CHECK(104)
        P4_SLEEP
        P4_ISSUE(104)
        P4_CHECK(120)
        "s_sub_u32 %[cnt], %[cnt], 1\n\t"
        "s_cbranch_scc0 1b\n\t"
        "2:\n\t"
        "s_mov_b64 exec, %[save]\n\t"
        : [r0] "=&v"(r[0]), [r1] "=&v"(r[1]), [r2] "=&v"(r[2]), [r3] "=&v"(r[3]), [r4] "=&v"(r[4]), [r5] "=&v"(r[5]), [r6] "=&v"(r[6]), [r7] "=&v"(r[7]),
          [save] "=&s"(save), [t0] "=&s"(t0), [t1] "=&s"(t1), [t2] "=&s"(t2), [cnt] "=&s"(cnt)
        : [voff] "v"(voff), [voff4] "v"(voff4), [rs] "s"(rs), [tag] "s"(tag)
        : "memory", "vcc", "scc", "v104", "v105", "v106", "v107", "v108", "v109", "v110", "v111", "v112", "v113", "v114", "v115", "v116", "v117", "v118", "v119",
          "v120", "v121", "v122", "v123", "v124", "v125", "v126", "v127", "v128", "v129", "v130", "v131", "v132", "v133", "v134", "v135");
}
__device__ __forceinline__ void poll2(__amdgpu_buffer_rsrc_t rs, int voff, int voff2, unsigned tag, int sel, float& r0, float& r1, float& r2, float& r3)
{
    unsigned long long save, t0, t1, t2; unsigned cnt;
    POLL_BODY("s_cbranch_scc0");
}

__global__ __launch_bounds__(512, 2) void geglu_decode_kernel(
    const unsigned short* __restrict__ x, const unsigned short* __restrict__ wg, const unsigned short* __restrict__ wu,
    const unsigned short* __restrict__ wd, unsigned short* out, unsigned char* ws)
{
    STAMP_DECL
    STAMP(0);
    __shared__ __attribute__((aligned(16))) float lds[2048];
    const int tid = threadIdx.x, l = tid & 63;
    const int w = __builtin_amdgcn_readfirstlane(tid >> 6);
    const int b = blockIdx.x;
    const int cb = b >> 3, kp = b & 7;

    const int mat = w >> 2, ww = w & 3;
    const __amdgpu_buffer_rsrc_t rs_gu = __builtin_amdgcn_make_buffer_rsrc((void*)(mat ? wu : wg), 0, DM * DF * 2, RSRC_W3);
    const __amdgpu_buffer_rsrc_t rs_d = __builtin_amdgcn_make_buffer_rsrc((void*)wd, 0, DM * DF * 2, RSRC_W3);
    const __amdgpu_buffer_rsrc_t rs_x = __builtin_amdgcn_make_buffer_rsrc((void*)x, 0, DM * 2, RSRC_W3);
    const __amdgpu_buffer_rsrc_t rs_ws = __builtin_amdgcn_make_buffer_rsrc((void*)ws, 0, WS_BYTES, RSRC_W3);
    const __amdgpu_buffer_rsrc_t rs_o = __builtin_amdgcn_make_buffer_rsrc((void*)out, 0, DM * 2, RSRC_W3);

    unsigned ep;
    asm volatile("s_load_dword %0, %1, 0x0" : "=s"(ep) : "s"(ws), "s"(x), "s"(wg), "s"(wu), "s"(wd), "s"(out) : "memory");
    const int q = l >> 4, n = l & 15;
    const int nun = (int)((U_PACK >> (4 * kp)) & 15u);
    const int base = 16 * (int)((B_PACK >> (8 * kp)) & 255ull);
    const int row0 = base + 4 * ww + q;
    const int voff = row0 * (DF * 2) + (128 * cb + 8 * n) * 2;
    v4u wv[NE];
    wv[0] = __builtin_amdgcn_raw_buffer_load_b128(rs_gu, voff, 0, AUX_NT);
    const unsigned xs = __builtin_amdgcn_raw_buffer_load_b16(rs_x, (n < nun) ? (row0 + 16 * n) * 2 : OOB, 0, 0);
#pragma unroll
    for (int i = 1; i < NE; ++i)
        wv[i] = __builtin_amdgcn_raw_buffer_load_b128(rs_gu, (i < UMIN || i < nun) ? voff : OOB, i * 16 * (DF * 2), AUX_NT);
    const int doff = (128 * cb + 16 * w + 4 * q) * (DM * 2) + (128 * kp + 8 * n) * 2;
    v4u dv[4];
    const int pub1 = MB1_OFF + (8 * cb + (tid >> 4)) * 2048 + kp * 256 + (tid & 15) * 16;
    const int pub2 = MB2_OFF + (8 * kp + (l >> 3)) * RED_BYTES + cb * 128 + (l & 7) * 16;
    __builtin_amdgcn_sched_barrier(0);
    asm volatile("s_waitcnt lgkmcnt(0)" : "+s"(ep) :: "memory");
    const unsigned tag = ep * 1664525u + 1013904223u;
    if (w < 2) {
        const v4u z = {0u, ~tag, 0u, ~tag};
        __builtin_amdgcn_raw_buffer_store_b128(z, rs_ws, pub1, 0, AUX_SC1);
        if (PREWRITE > 1 && w == 0) __builtin_amdgcn_raw_buffer_store_b128(z, rs_ws, pub2, 0, AUX_SC1);
    }
    __builtin_amdgcn_sched_barrier(0);
    STAMP(1);

    float acc[8];
#pragma unroll
    for (int k = 0; k < 8; ++k) acc[k] = 0.f;
    const int xf = (int)(xs << 16);
#define FMA_UNIT(i) if constexpr ((i) < NE) { \
        const float xi = __builtin_bit_cast(float, __builtin_amdgcn_update_dpp(0, xf, 0x150 + (i), 0xf, 0xf, false));   \
        _Pragma("unroll") for (int k = 0; k < 4; ++k) { \
            acc[2 * k] = fmaf(xi, bf_lo(wv[i][k]), acc[2 * k]); \
            acc[2 * k + 1] = fmaf(xi, bf_hi(wv[i][k]), acc[2 * k + 1]); } }
    FMA_UNIT(0) FMA_UNIT(1) FMA_UNIT(2) FMA_UNIT(3) FMA_UNIT(4) FMA_UNIT(5) FMA_UNIT(6) FMA_UNIT(7) FMA_UNIT(8) FMA_UNIT(9) FMA_UNIT(10) FMA_UNIT(11)
#undef FMA_UNIT
    asm volatile("" :: "v"(wv[NE - 1]));
    __builtin_amdgcn_sched_barrier(0);
#pragma unroll
    for (int k = 0; k < 4; ++k) dv[k] = __builtin_amdgcn_raw_buffer_load_b128(rs_d, doff, k * (DM * 2), AUX_NT);
    __builtin_amdgcn_sched_barrier(0);
    STAMP(2);
    {
        const float b0 = swap32_add(acc[0], acc[4]), b1 = swap32_add(acc[1], acc[5]);
        const float b2 = swap32_add(acc[2], acc[6]), b3 = swap32_add(acc[3], acc[7]);
        v2f c; c[0] = swap16_add(b0, b2); c[1] = swap16_add(b1, b3);
        *(v2f*)&lds[w * 128 + 8 * n + 2 * q] = c;
    }
    const unsigned msk = ((n & 3) < 2) ? ((n & 1) ? 0xffff0000u : 0x0000ffffu) : 0u;
    unsigned mk[4];
#pragma unroll
    for (int r = 0; r < 4; ++r) mk[r] = ((n >> 2) == r) ? msk : 0u;
    int off1 = MB1_OFF + (8 * cb + w) * 2048 + ((l >> 2) & 3) * 256 + (4 * q + (l & 3)) * 16, off1b = off1 + 1024;
    asm volatile("" : "+v"(mk[0]), "+v"(mk[1]), "+v"(mk[2]), "+v"(mk[3]), "+v"(off1), "+v"(off1b));
    __syncthreads();
    STAMP(3);
    if (w < 2) {
        const float g = (lds[tid] + lds[128 + tid]) + (lds[256 + tid] + lds[384 + tid]);
        const float u = (lds[512 + tid] + lds[640 + tid]) + (lds[768 + tid] + lds[896 + tid]);
        const v4u p = {f2u(g), tag, f2u(u), tag};
        asm volatile("s_waitcnt vmcnt(4)" ::: "memory");
        __builtin_amdgcn_raw_buffer_store_b128(p, rs_ws, pub1, 0, AUX_SC1);
    }
    STAMP(4);
#define H1_WAIT 14
    if (H1_WAIT) __builtin_amdgcn_s_sleep(H1_WAIT);
    float p0, p1, p2, p3;
#define H1_SLP 0
    poll1<H1_SLP>(rs_ws, off1, off1b, tag, p0, p1, p2, p3);
    STAMP(5);
    unsigned hpk;
    {
        float g = p0 + p2, u = p1 + p3;
        g = dpp_add<DPP_ROR(4)>(g); u = dpp_add<DPP_ROR(4)>(u);
        g = dpp_add<DPP_ROR(8)>(g); u = dpp_add<DPP_ROR(8)>(u);
        g = rbf(g); u = rbf(u);
        const float t = g * fmaf(g * g, -0.10294323970f, -2.30220819814f);
        const float ge = rbf(g * __builtin_amdgcn_rcpf(1.0f + __builtin_amdgcn_exp2f(t)));
        const unsigned hb = f2bf(ge * u);
        hpk = hb | (hb << 16);
    }
    v4f o = {0.f, 0.f, 0.f, 0.f};
#define STEP(i) { const unsigned hh = (unsigned)__builtin_amdgcn_update_dpp(0, (int)hpk, DPP_QUAD(i), 0xf, 0xf, true); \
        const v4u a = {hh & mk[0], hh & mk[1], hh & mk[2], hh & mk[3]}; \
        o = __builtin_amdgcn_mfma_f32_16x16x32_bf16(__builtin_bit_cast(v8s, a), __builtin_bit_cast(v8s, dv[i]), o, 0, 0, 0); }
    STEP(0) STEP(1) STEP(2) STEP(3)
#undef STEP
    {
        v2f c; c[0] = o[0]; c[1] = o[1];
        *(v2f*)&lds[1024 + w * 128 + 8 * n + 2 * q] = c;
    }
    __syncthreads();
    STAMP(6);
    if (w == 0) {
        v2f s[8];
#pragma unroll
        for (int k = 0; k < 8; ++k) s[k] = *(const v2f*)&lds[1024 + k * 128 + 2 * l];
        const v2f t = ((s[0] + s[1]) + (s[2] + s[3])) + ((s[4] + s[5]) + (s[6] + s[7]));
        const v4u p = {f2u(t[0]), tag, f2u(t[1]), tag};
        __builtin_amdgcn_raw_buffer_store_b128(p, rs_ws, pub2, 0, AUX_SC1);
    }
    STAMP(7);

    constexpr int FW = 0;
    if (b >= NRED || w != FW) { STAMP_FLUSH(); return; }
    {
        const int o0 = MB2_OFF + b * RED_BYTES + l * 16;
        float v[8];
        poll4x2(rs_ws, o0, l < 32 ? o0 + 3072 : o0, tag, v);
        STAMP(8);
        const float s0 = ((v[0] + v[2]) + v[4]) + (l < 32 ? v[6] : 0.f), s1 = ((v[1] + v[3]) + v[5]) + (l < 32 ? v[7] : 0.f);
        float s = swap32_add(s0, s1);
        s = swap16_add(s, s);
        s = dpp_add<DPP_ROR(8)>(s);
        if ((l & 24) == 0) __builtin_amdgcn_raw_buffer_store_b16((unsigned short)f2bf(s), rs_o, (16 * b + 2 * (l & 7) + (l >> 5)) * 2, 0, 0);
        if (b == 0 && l == 0) __hip_atomic_store((gu32*)ws, tag, RLX_AGENT);
    }
    STAMP(9);
    STAMP_FLUSH();
}

extern "C" void kernel_launch(void* const* d_in, const int* in_sizes, int n_in,
                              void* d_out, int out_size, void* d_ws, size_t ws_size,
                              hipStream_t stream) {
    geglu_decode_kernel<<<NWG, 512, 0, stream>>>((const unsigned short*)d_in[0], (const unsigned short*)d_in[1],
        (const unsigned short*)d_in[2], (const unsigned short*)d_in[3], (unsigned short*)d_out, (unsigned char*)d_ws);
}
```

```cpp
#include <hip/hip_runtime.h>
#include <hip/hip_bf16.h>
#include <stdint.h>

typedef unsigned v4u __attribute__((ext_vector_type(4)));
typedef float v4f __attribute__((ext_vector_type(4)));
typedef float v2f __attribute__((ext_vector_type(2)));
typedef short v8s __attribute__((ext_vector_type(8)));
#define GAS __attribute__((address_space(1)))
typedef GAS unsigned gu32;
#define RLX_AGENT __ATOMIC_RELAXED, __HIP_MEMORY_SCOPE_AGENT

constexpr int DM = 1024, DF = 3584;
constexpr int NGRP = 28;
constexpr int NWG = 8 * NGRP;
constexpr int NRED = 64;
constexpr int MB1_OFF = 4096, BOX1 = 2048;
constexpr int MB2_OFF = MB1_OFF + NWG * BOX1, BOX2 = 4096;
constexpr int WS_BYTES = MB2_OFF + NRED * BOX2;
constexpr int AUX_NT = 2, AUX_SC1 = 16;
constexpr int RSRC_W3 = 0x00020000;
constexpr int OOB = 0x40000000;

#define UNITS 9, 8, 8, 8, 7, 8, 8, 8
constexpr int UN[8] = {UNITS};
constexpr int umax() { int m = 0; for (int i = 0; i < 8; ++i) m = UN[i] > m ? UN[i] : m; return m; }
constexpr int umin() { int m = 99; for (int i = 0; i < 8; ++i) m = UN[i] < m ? UN[i] : m; return m; }
constexpr unsigned upack() { unsigned p = 0; for (int i = 0; i < 8; ++i) p |= (unsigned)UN[i] << (4 * i); return p; }
constexpr unsigned long long bpack() { unsigned long long p = 0; int b = 0; for (int i = 0; i < 8; ++i) { p |= (unsigned long long)b << (8 * i); b += UN[i]; } return p; }
constexpr int usum() { int b = 0; for (int i = 0; i < 8; ++i) b += UN[i]; return b; }
static_assert(usum() == 64 && umax() <= 12, "the units must cover the 1024 rows");
constexpr int NE = umax(), UMIN = umin();
constexpr unsigned U_PACK = upack();
constexpr unsigned long long B_PACK = bpack();
#define H1_WAIT 6

__device__ __forceinline__ float u2f(unsigned v) { return __builtin_bit_cast(float, v); }
__device__ __forceinline__ unsigned f2u(float v) { return __builtin_bit_cast(unsigned, v); }
__device__ __forceinline__ float bf_lo(unsigned v) { return __builtin_bit_cast(float, v << 16); }
__device__ __forceinline__ float bf_hi(unsigned v) { return __builtin_bit_cast(float, v & 0xffff0000u); }
__device__ __forceinline__ unsigned f2bf(float f) {
    __hip_bfloat16 b = __float2bfloat16(f);
    return (unsigned)__builtin_bit_cast(unsigned short, b);
}
__device__ __forceinline__ float rbf(float f) { return __builtin_bit_cast(float, f2bf(f) << 16); }
template <int CTRL> __device__ __forceinline__ float dpp_add(float v) {
    int t = __builtin_amdgcn_update_dpp(0, __builtin_bit_cast(int, v), CTRL, 0xf, 0xf, true);
    return v + __builtin_bit_cast(float, t);
}
__device__ __forceinline__ float swap32_add(float a, float b) {
    auto r = __builtin_amdgcn_permlane32_swap(f2u(a), f2u(b), false, false);
    return u2f(r[0]) + u2f(r[1]);
}
__device__ __forceinline__ float swap16_add(float a, float b) {
    auto r = __builtin_amdgcn_permlane16_swap(f2u(a), f2u(b), false, false);
    return u2f(r[0]) + u2f(r[1]);
}
constexpr int DPP_ROR(int n) { return 0x120 + n; }
constexpr int DPP_QUAD(int i) { return i | (i << 2) | (i << 4) | (i << 6); }

__device__ __forceinline__ void poll2(__amdgpu_buffer_rsrc_t rs, int voff, unsigned tag, float& r0, float& r1, float& r2, float& r3)
{
    unsigned long long save, t0, t1, t2; unsigned cnt;
    asm volatile(
        "s_mov_b64 %[save], exec\n\t"
        "s_mov_b32 %[cnt], 0x100000\n\t"
        "s_nop 3\n\t"
        "1:\n\t"
        "buffer_load_dwordx4 v[64:67], %[voff], %[rs], 0 offen sc1\n\t"
        "buffer_load_dwordx4 v[68:71], %[voff], %[rs], 0 offen offset:1024 sc1\n\t"
        "s_waitcnt vmcnt(0)\n\t"
        "v_cmp_ne_u32_e32 vcc, %[tag], v65\n\t"
        "v_cmp_ne_u32_e64 %[t0], %[tag], v67\n\t"
        "v_cmp_ne_u32_e64 %[t1], %[tag], v69\n\t"
        "v_cmp_ne_u32_e64 %[t2], %[tag], v71\n\t"
        "s_or_b64 vcc, vcc, %[t0]\n\t"
        "s_or_b64 %[t1], %[t1], %[t2]\n\t"
        "s_or_b64 exec, vcc, %[t1]\n\t"
        "s_cbranch_execz 2f\n\t"
        "s_sub_u32 %[cnt], %[cnt], 1\n\t"
        "s_cbranch_scc0 1b\n\t"
        "2:\n\t"
        "s_mov_b64 exec, %[save]\n\t"
        "v_mov_b32 %[r0], v64\n\t"
        "v_mov_b32 %[r1], v66\n\t"
        "v_mov_b32 %[r2], v68\n\t"
        "v_mov_b32 %[r3], v70\n\t"
        : [r0] "=&v"(r0), [r1] "=&v"(r1), [r2] "=&v"(r2), [r3] "=&v"(r3), [save] "=&s"(save), [t0] "=&s"(t0), [t1] "=&s"(t1), [t2] "=&s"(t2), [cnt] "=&s"(cnt)
        : [voff] "v"(voff), [rs] "s"(rs), [tag] "s"(tag)
        : "memory", "vcc", "scc", "v64", "v65", "v66", "v67", "v68", "v69", "v70", "v71");
}
#define P4_ISSUE(B) \
        "buffer_load_dwordx4 v[" #B "+0:" #B "+3], %[voff], %[rs], 0 offen sc1\n\t" \
        "buffer_load_dwordx4 v[" #B "+4:" #B "+7], %[voff], %[rs], 0 offen offset:1024 sc1\n\t" \
        "buffer_load_dwordx4 v[" #B "+8:" #B "+11], %[voff], %[rs], 0 offen offset:2048 sc1\n\t" \
        "buffer_load_dwordx4 v[" #B "+12:" #B "+15], %[voff4], %[rs], 0 offen sc1\n\t"
#define P4_CHECK(B) \
        "s_waitcnt vmcnt(4)\n\t" \
        "v_cmp_ne_u32_e32 vcc, %[tag], v[" #B "+1]\n\t" \
        "v_cmp_ne_u32_e64 %[t0], %[tag], v[" #B "+3]\n\t" \
        "v_cmp_ne_u32_e64 %[t1], %[tag], v[" #B "+5]\n\t" \
        "v_cmp_ne_u32_e64 %[t2], %[tag], v[" #B "+7]\n\t" \
        "s_or_b64 vcc, vcc, %[t0]\n\t" \
        "s_or_b64 %[t1], %[t1], %[t2]\n\t" \
        "s_or_b64 %[t0], vcc, %[t1]\n\t" \
        "v_cmp_ne_u32_e32 vcc, %[tag], v[" #B "+9]\n\t" \
        "v_cmp_ne_u32_e64 %[t1], %[tag], v[" #B "+11]\n\t" \
        "s_or_b64 %[t0], %[t0], vcc\n\t" \
        "v_cmp_ne_u32_e32 vcc, %[tag], v[" #B "+13]\n\t" \
        "v_cmp_ne_u32_e64 %[t2], %[tag], v[" #B "+15]\n\t" \
        "s_or_b64 %[t0], %[t0], %[t1]\n\t" \
        "s_or_b64 vcc, vcc, %[t2]\n\t" \
        "s_or_b64 %[t0], vcc, %[t0]\n\t" \
        "s_andn2_b64 exec, exec, %[t0]\n\t" \
        "v_mov_b32 %[r0], v[" #B "+0]\n\t" \
        "v_mov_b32 %[r1], v[" #B "+2]\n\t" \
        "v_mov_b32 %[r2], v[" #B "+4]\n\t" \
        "v_mov_b32 %[r3], v[" #B "+6]\n\t" \
        "v_mov_b32 %[r4], v[" #B "+8]\n\t" \
        "v_mov_b32 %[r5], v[" #B "+10]\n\t" \
        "v_mov_b32 %[r6], v[" #B "+12]\n\t" \
        "v_mov_b32 %[r7], v[" #B "+14]\n\t" \
        "s_mov_b64 exec, %[t0]\n\t" \
        "s_cmp_eq_u64 %[t0], 0\n\t" \
        "s_cbranch_scc1 2f\n\t"
__device__ __forceinline__ void poll4(__amdgpu_buffer_rsrc_t rs, int voff, int voff4, unsigned tag, float (&r)[8])
{
    unsigned long long save, t0, t1, t2; unsigned cnt;
    asm volatile(
        "s_mov_b64 %[save], exec\n\t"
        "s_mov_b32 %[cnt], 0x80000\n\t"
        "s_nop 3\n\t"
        P4_ISSUE(64)
        "1:\n\t"
        "s_sleep 2\n\t"
        P4_ISSUE(80)
        P4_CHECK(64)
        "s_sleep 2\n\t"
        P4_ISSUE(64)
        P4_CHECK(80)
        "s_sub_u32 %[cnt], %[cnt], 1\n\t"
        "s_cbranch_scc0 1b\n\t"
        "2:\n\t"
        "s_mov_b64 exec, %[save]\n\t"
        : [r0] "=&v"(r[0]), [r1] "=&v"(r[1]), [r2] "=&v"(r[2]), [r3] "=&v"(r[3]), [r4] "=&v"(r[4]), [r5] "=&v"(r[5]), [r6] "=&v"(r[6]), [r7] "=&v"(r[7]),
          [save] "=&s"(save), [t0] "=&s"(t0), [t1] "=&s"(t1), [t2] "=&s"(t2), [cnt] "=&s"(cnt)
        : [voff] "v"(voff), [voff4] "v"(voff4), [rs] "s"(rs), [tag] "s"(tag)
        : "memory", "vcc", "scc", "v64", "v65", "v66", "v67", "v68", "v69", "v70", "v71", "v72", "v73", "v74", "v75", "v76", "v77", "v78", "v79",
          "v80", "v81", "v82", "v83", "v84", "v85", "v86", "v87", "v88", "v89", "v90", "v91", "v92", "v93", "v94", "v95");
}

__global__ __launch_bounds__(512, 2) void standalone__glu_gated_geglu__decode_kernel(
    const unsigned short* __restrict__ x, const unsigned short* __restrict__ wg, const unsigned short* __restrict__ wu,
    const unsigned short* __restrict__ wd, unsigned short* out, unsigned char* ws)
{
    __shared__ __attribute__((aligned(16))) float lds[2048];
    const int tid = threadIdx.x, l = tid & 63;
    const int w = __builtin_amdgcn_readfirstlane(tid >> 6);
    const int b = blockIdx.x;
    const int cb = b >> 3, kp = b & 7;
    const int mat = w >> 2, ww = w & 3;
    const int q = l >> 4, n = l & 15;

    const int nun = (int)((U_PACK >> (4 * kp)) & 15u);
    const int base = 16 * (int)((B_PACK >> (8 * kp)) & 255ull);
    const int row0 = base + 4 * ww + q;
    int voff = row0 * (DF * 2) + (128 * cb + 8 * n) * 2;
    int xoff = (n < nun) ? (row0 + 16 * n) * 2 : OOB;
    asm volatile("" : "+v"(voff), "+v"(xoff));
    __builtin_amdgcn_sched_barrier(0);

    const __amdgpu_buffer_rsrc_t rs_gu = __builtin_amdgcn_make_buffer_rsrc((void*)(mat ? wu : wg), 0, DM * DF * 2, RSRC_W3);
    const __amdgpu_buffer_rsrc_t rs_x = __builtin_amdgcn_make_buffer_rsrc((void*)x, 0, DM * 2, RSRC_W3);
    v4u wv[NE];
    wv[0] = __builtin_amdgcn_raw_buffer_load_b128(rs_gu, voff, 0, AUX_NT);
    const unsigned xs = __builtin_amdgcn_raw_buffer_load_b16(rs_x, xoff, 0, 0);
#pragma unroll
    for (int i = 1; i < NE; ++i)
        wv[i] = __builtin_amdgcn_raw_buffer_load_b128(rs_gu, (i < UMIN || i < nun) ? voff : OOB, i * 16 * (DF * 2), AUX_NT);
    __builtin_amdgcn_sched_barrier(0);
    unsigned ep;
    asm volatile("s_load_dword %0, %1, 0x0" : "=s"(ep) : "s"(ws) : "memory");
    const __amdgpu_buffer_rsrc_t rs_d = __builtin_amdgcn_make_buffer_rsrc((void*)wd, 0, DM * DF * 2, RSRC_W3);
    const __amdgpu_buffer_rsrc_t rs_ws = __builtin_amdgcn_make_buffer_rsrc((void*)ws, 0, WS_BYTES, RSRC_W3);
    const __amdgpu_buffer_rsrc_t rs_o = __builtin_amdgcn_make_buffer_rsrc((void*)out, 0, DM * 2, RSRC_W3);
    const int doff = (128 * cb + 16 * w + 4 * q) * (DM * 2) + (128 * kp + 8 * n) * 2;
    const int pub1 = MB1_OFF + (8 * cb + (tid >> 4)) * BOX1 + kp * 256 + (tid & 15) * 16;
    const int pub2 = MB2_OFF + (8 * kp + (l >> 3)) * BOX2 + cb * 128 + (l & 7) * 16;
    asm volatile("s_waitcnt lgkmcnt(0)" : "+s"(ep) :: "memory");
    const unsigned tag = ep * 1664525u + 1013904223u;
    if (w < 2) {
        const v4u z = {0u, ~tag, 0u, ~tag};
        __builtin_amdgcn_raw_buffer_store_b128(z, rs_ws, pub1, 0, AUX_SC1);
        if (w == 0) __builtin_amdgcn_raw_buffer_store_b128(z, rs_ws, pub2, 0, AUX_SC1);
    }
    __builtin_amdgcn_sched_barrier(0);

    float acc[8];
#pragma unroll
    for (int k = 0; k < 8; ++k) acc[k] = 0.f;
    const int xf = (int)(xs << 16);
#define FMA_UNIT(i) if constexpr ((i) < NE) { \
        const float xi = __builtin_bit_cast(float, __builtin_amdgcn_update_dpp(0, xf, 0x150 + (i), 0xf, 0xf, false));   \
        _Pragma("unroll") for (int k = 0; k < 4; ++k) { \
            acc[2 * k] = fmaf(xi, bf_lo(wv[i][k]), acc[2 * k]); \
            acc[2 * k + 1] = fmaf(xi, bf_hi(wv[i][k]), acc[2 * k + 1]); } }
    FMA_UNIT(0) FMA_UNIT(1) FMA_UNIT(2) FMA_UNIT(3) FMA_UNIT(4) FMA_UNIT(5) FMA_UNIT(6) FMA_UNIT(7) FMA_UNIT(8) FMA_UNIT(9) FMA_UNIT(10) FMA_UNIT(11)
#undef FMA_UNIT
    asm volatile("" :: "v"(wv[NE - 1]));
    __builtin_amdgcn_sched_barrier(0);
    v4u dv[4];
#pragma unroll
    for (int k = 0; k < 4; ++k) dv[k] = __builtin_amdgcn_raw_buffer_load_b128(rs_d, doff, k * (DM * 2), AUX_NT);
    __builtin_amdgcn_sched_barrier(0);
    {
        const float b0 = swap32_add(acc[0], acc[4]), b1 = swap32_add(acc[1], acc[5]);
        const float b2 = swap32_add(acc[2], acc[6]), b3 = swap32_add(acc[3], acc[7]);
        v2f c; c[0] = swap16_add(b0, b2); c[1] = swap16_add(b1, b3);
        *(v2f*)&lds[w * 128 + 8 * n + 2 * q] = c;
    }
    const unsigned msk = ((n & 3) < 2) ? ((n & 1) ? 0xffff0000u : 0x0000ffffu) : 0u;
    unsigned mk[4];
#pragma unroll
    for (int r = 0; r < 4; ++r) mk[r] = ((n >> 2) == r) ? msk : 0u;
    int off1 = MB1_OFF + (8 * cb + w) * BOX1 + ((l >> 2) & 3) * 256 + (4 * q + (l & 3)) * 16;
    asm volatile("" : "+v"(mk[0]), "+v"(mk[1]), "+v"(mk[2]), "+v"(mk[3]), "+v"(off1));
    __syncthreads();
    if (w < 2) {
        const float g = (lds[tid] + lds[128 + tid]) + (lds[256 + tid] + lds[384 + tid]);
        const float u = (lds[512 + tid] + lds[640 + tid]) + (lds[768 + tid] + lds[896 + tid]);
        const v4u p = {f2u(g), tag, f2u(u), tag};
        asm volatile("s_waitcnt vmcnt(4)" ::: "memory");
        __builtin_amdgcn_raw_buffer_store_b128(p, rs_ws, pub1, 0, AUX_SC1);
    }
    __builtin_amdgcn_s_sleep(H1_WAIT);
    float p0, p1, p2, p3;
    poll2(rs_ws, off1, tag, p0, p1, p2, p3);
    unsigned hpk;
    {
        float g = p0 + p2, u = p1 + p3;
        g = dpp_add<DPP_ROR(4)>(g); u = dpp_add<DPP_ROR(4)>(u);
        g = dpp_add<DPP_ROR(8)>(g); u = dpp_add<DPP_ROR(8)>(u);
        g = rbf(g); u = rbf(u);
        const float t = g * fmaf(g * g, -0.10294323970f, -2.30220819814f);
        const float ge = rbf(g * __builtin_amdgcn_rcpf(1.0f + __builtin_amdgcn_exp2f(t)));
        const unsigned hb = f2bf(ge * u);
        hpk = hb | (hb << 16);
    }
    v4f o = {0.f, 0.f, 0.f, 0.f};
#define STEP(i) { const unsigned hh = (unsigned)__builtin_amdgcn_update_dpp(0, (int)hpk, DPP_QUAD(i), 0xf, 0xf, true); \
        const v4u a = {hh & mk[0], hh & mk[1], hh & mk[2], hh & mk[3]}; \
        o = __builtin_amdgcn_mfma_f32_16x16x32_bf16(__builtin_bit_cast(v8s, a), __builtin_bit_cast(v8s, dv[i]), o, 0, 0, 0); }
    STEP(0) STEP(1) STEP(2) STEP(3)
#undef STEP
    {
        v2f c; c[0] = o[0]; c[1] = o[1];
        *(v2f*)&lds[1024 + w * 128 + 8 * n + 2 * q] = c;
    }
    __syncthreads();
    if (w != 0) return;
    {
        v2f s[8];
#pragma unroll
        for (int k = 0; k < 8; ++k) s[k] = *(const v2f*)&lds[1024 + k * 128 + 2 * l];
        const v2f t = ((s[0] + s[1]) + (s[2] + s[3])) + ((s[4] + s[5]) + (s[6] + s[7]));
        const v4u p = {f2u(t[0]), tag, f2u(t[1]), tag};
        __builtin_amdgcn_raw_buffer_store_b128(p, rs_ws, pub2, 0, AUX_SC1);
    }
    if (b >= NRED) return;

    {
        const int o0 = MB2_OFF + b * BOX2 + l * 16;
        float v[8];
        poll4(rs_ws, o0, l < 32 ? o0 + 3072 : o0, tag, v);
        const float s0 = ((v[0] + v[2]) + v[4]) + (l < 32 ? v[6] : 0.f), s1 = ((v[1] + v[3]) + v[5]) + (l < 32 ? v[7] : 0.f);
        float s = swap32_add(s0, s1);
        s = swap16_add(s, s);
        s = dpp_add<DPP_ROR(8)>(s);
        if ((l & 24) == 0) __builtin_amdgcn_raw_buffer_store_b16((unsigned short)f2bf(s), rs_o, (16 * b + 2 * (l & 7) + (l >> 5)) * 2, 0, 0);
        if (b == 0 && l == 0) __hip_atomic_store((gu32*)ws, tag, RLX_AGENT);
    }
}

extern "C" void kernel_launch(void* const* d_in, const int* in_sizes, int n_in,
                              void* d_out, int out_size, void* d_ws, size_t ws_size,
                              hipStream_t stream) {
    standalone__glu_gated_geglu__decode_kernel<<<NWG, 512, 0, stream>>>((const unsigned short*)d_in[0], (const unsigned short*)d_in[1],
        (const unsigned short*)d_in[2], (const unsigned short*)d_in[3], (unsigned short*)d_out, (unsigned char*)d_ws);
}
```

```cpp
#include <hip/hip_runtime.h>
#include <hip/hip_bf16.h>
#include <stdint.h>

typedef unsigned v4u __attribute__((ext_vector_type(4)));
typedef float v4f __attribute__((ext_vector_type(4)));
typedef float v2f __attribute__((ext_vector_type(2)));
typedef short v8s __attribute__((ext_vector_type(8)));
#define GAS __attribute__((address_space(1)))
typedef GAS unsigned gu32;
#define RLX_AGENT __ATOMIC_RELAXED, __HIP_MEMORY_SCOPE_AGENT

constexpr int DM = 1024, DF = 3584;
constexpr int NGRP = 28;
constexpr int NWG = 8 * NGRP;
constexpr int NRED = 64;
constexpr int MB1_OFF = 4096, BOX1 = 2048;
constexpr int MB2_OFF = MB1_OFF + NWG * BOX1, BOX2 = 4096;
constexpr int WS_BYTES = MB2_OFF + NRED * BOX2;
constexpr int AUX_NT = 2, AUX_SC1 = 16;
constexpr int RSRC_W3 = 0x00020000;
constexpr int OOB = 0x40000000;

#define UNITS 9, 8, 8, 8, 7, 8, 8, 8
constexpr int UN[8] = {UNITS};
constexpr int umax() { int m = 0; for (int i = 0; i < 8; ++i) m = UN[i] > m ? UN[i] : m; return m; }
constexpr int umin() { int m = 99; for (int i = 0; i < 8; ++i) m = UN[i] < m ? UN[i] : m; return m; }
constexpr unsigned upack() { unsigned p = 0; for (int i = 0; i < 8; ++i) p |= (unsigned)UN[i] << (4 * i); return p; }
constexpr unsigned long long bpack() { unsigned long long p = 0; int b = 0; for (int i = 0; i < 8; ++i) { p |= (unsigned long long)b << (8 * i); b += UN[i]; } return p; }
constexpr int usum() { int b = 0; for (int i = 0; i < 8; ++i) b += UN[i]; return b; }
static_assert(usum() == 64 && umax() <= 12, "the units must cover the 1024 rows");
constexpr int NE = umax(), UMIN = umin();
constexpr unsigned U_PACK = upack();
constexpr unsigned long long B_PACK = bpack();
#define H1_WAIT 14

__device__ __forceinline__ float u2f(unsigned v) { return __builtin_bit_cast(float, v); }
__device__ __forceinline__ unsigned f2u(float v) { return __builtin_bit_cast(unsigned, v); }
__device__ __forceinline__ float bf_lo(unsigned v) { return __builtin_bit_cast(float, v << 16); }
__device__ __forceinline__ float bf_hi(unsigned v) { return __builtin_bit_cast(float, v & 0xffff0000u); }
__device__ __forceinline__ unsigned f2bf(float f) {
    __hip_bfloat16 b = __float2bfloat16(f);
    return (unsigned)__builtin_bit_cast(unsigned short, b);
}
__device__ __forceinline__ float rbf(float f) { return __builtin_bit_cast(float, f2bf(f) << 16); }
template <int CTRL> __device__ __forceinline__ float dpp_add(float v) {
    int t = __builtin_amdgcn_update_dpp(0, __builtin_bit_cast(int, v), CTRL, 0xf, 0xf, true);
    return v + __builtin_bit_cast(float, t);
}
__device__ __forceinline__ float swap32_add(float a, float b) {
    auto r = __builtin_amdgcn_permlane32_swap(f2u(a), f2u(b), false, false);
    return u2f(r[0]) + u2f(r[1]);
}
__device__ __forceinline__ float swap16_add(float a, float b) {
    auto r = __builtin_amdgcn_permlane16_swap(f2u(a), f2u(b), false, false);
    return u2f(r[0]) + u2f(r[1]);
}
constexpr int DPP_ROR(int n) { return 0x120 + n; }
constexpr int DPP_QUAD(int i) { return i | (i << 2) | (i << 4) | (i << 6); }

__device__ __forceinline__ void poll2(__amdgpu_buffer_rsrc_t rs, int voff, unsigned tag, float& r0, float& r1, float& r2, float& r3)
{
    unsigned long long save, t0, t1, t2; unsigned cnt;
    asm volatile(
        "s_mov_b64 %[save], exec\n\t"
        "s_mov_b32 %[cnt], 0x100000\n\t"
        "s_nop 3\n\t"
        "1:\n\t"
        "buffer_load_dwordx4 v[64:67], %[voff], %[rs], 0 offen sc1\n\t"
        "buffer_load_dwordx4 v[68:71], %[voff], %[rs], 0 offen offset:1024 sc1\n\t"
        "s_waitcnt vmcnt(0)\n\t"
        "v_cmp_ne_u32_e32 vcc, %[tag], v65\n\t"
        "v_cmp_ne_u32_e64 %[t0], %[tag], v67\n\t"
        "v_cmp_ne_u32_e64 %[t1], %[tag], v69\n\t"
        "v_cmp_ne_u32_e64 %[t2], %[tag], v71\n\t"
        "s_or_b64 vcc, vcc, %[t0]\n\t"
        "s_or_b64 %[t1], %[t1], %[t2]\n\t"
        "s_or_b64 exec, vcc, %[t1]\n\t"
        "s_cbranch_execz 2f\n\t"
        "s_sub_u32 %[cnt], %[cnt], 1\n\t"
        "s_cbranch_scc0 1b\n\t"
        "2:\n\t"
        "s_mov_b64 exec, %[save]\n\t"
        "v_mov_b32 %[r0], v64\n\t"
        "v_mov_b32 %[r1], v66\n\t"
        "v_mov_b32 %[r2], v68\n\t"
        "v_mov_b32 %[r3], v70\n\t"
        : [r0] "=&v"(r0), [r1] "=&v"(r1), [r2] "=&v"(r2), [r3] "=&v"(r3), [save] "=&s"(save), [t0] "=&s"(t0), [t1] "=&s"(t1), [t2] "=&s"(t2), [cnt] "=&s"(cnt)
        : [voff] "v"(voff), [rs] "s"(rs), [tag] "s"(tag)
        : "memory", "vcc", "scc", "v64", "v65", "v66", "v67", "v68", "v69", "v70", "v71");
}
#define P4_ISSUE(B) \
        "buffer_load_dwordx4 v[" #B "+0:" #B "+3], %[voff], %[rs], 0 offen sc1\n\t" \
        "buffer_load_dwordx4 v[" #B "+4:" #B "+7], %[voff], %[rs], 0 offen offset:1024 sc1\n\t" \
        "buffer_load_dwordx4 v[" #B "+8:" #B "+11], %[voff], %[rs], 0 offen offset:2048 sc1\n\t" \
        "buffer_load_dwordx4 v[" #B "+12:" #B "+15], %[voff4], %[rs], 0 offen sc1\n\t"
#define P4_CHECK(B) \
        "s_waitcnt vmcnt(4)\n\t" \
        "v_cmp_ne_u32_e32 vcc, %[tag], v[" #B "+1]\n\t" \
        "v_cmp_ne_u32_e64 %[t0], %[tag], v[" #B "+3]\n\t" \
        "v_cmp_ne_u32_e64 %[t1], %[tag], v[" #B "+5]\n\t" \
        "v_cmp_ne_u32_e64 %[t2], %[tag], v[" #B "+7]\n\t" \
        "s_or_b64 vcc, vcc, %[t0]\n\t" \
        "s_or_b64 %[t1], %[t1], %[t2]\n\t" \
        "s_or_b64 %[t0], vcc, %[t1]\n\t" \
        "v_cmp_ne_u32_e32 vcc, %[tag], v[" #B "+9]\n\t" \
        "v_cmp_ne_u32_e64 %[t1], %[tag], v[" #B "+11]\n\t" \
        "s_or_b64 %[t0], %[t0], vcc\n\t" \
        "v_cmp_ne_u32_e32 vcc, %[tag], v[" #B "+13]\n\t" \
        "v_cmp_ne_u32_e64 %[t2], %[tag], v[" #B "+15]\n\t" \
        "s_or_b64 %[t0], %[t0], %[t1]\n\t" \
        "s_or_b64 vcc, vcc, %[t2]\n\t" \
        "s_or_b64 %[t0], vcc, %[t0]\n\t" \
        "s_andn2_b64 exec, exec, %[t0]\n\t" \
        "v_mov_b32 %[r0], v[" #B "+0]\n\t" \
        "v_mov_b32 %[r1], v[" #B "+2]\n\t" \
        "v_mov_b32 %[r2], v[" #B "+4]\n\t" \
        "v_mov_b32 %[r3], v[" #B "+6]\n\t" \
        "v_mov_b32 %[r4], v[" #B "+8]\n\t" \
        "v_mov_b32 %[r5], v[" #B "+10]\n\t" \
        "v_mov_b32 %[r6], v[" #B "+12]\n\t" \
        "v_mov_b32 %[r7], v[" #B "+14]\n\t" \
        "s_mov_b64 exec, %[t0]\n\t" \
        "s_cmp_eq_u64 %[t0], 0\n\t" \
        "s_cbranch_scc1 2f\n\t"
__device__ __forceinline__ void poll4(__amdgpu_buffer_rsrc_t rs, int voff, int voff4, unsigned tag, float (&r)[8])
{
    unsigned long long save, t0, t1, t2; unsigned cnt;
    asm volatile(
        "s_mov_b64 %[save], exec\n\t"
        "s_mov_b32 %[cnt], 0x80000\n\t"
        "s_nop 3\n\t"
        P4_ISSUE(64)
        "1:\n\t"
        "s_sleep 2\n\t"
        P4_ISSUE(80)
        P4_CHECK(64)
        "s_sleep 2\n\t"
        P4_ISSUE(64)
        P4_CHECK(80)
        "s_sub_u32 %[cnt], %[cnt], 1\n\t"
        "s_cbranch_scc0 1b\n\t"
        "2:\n\t"
        "s_mov_b64 exec, %[save]\n\t"
        : [r0] "=&v"(r[0]), [r1] "=&v"(r[1]), [r2] "=&v"(r[2]), [r3] "=&v"(r[3]), [r4] "=&v"(r[4]), [r5] "=&v"(r[5]), [r6] "=&v"(r[6]), [r7] "=&v"(r[7]),
          [save] "=&s"(save), [t0] "=&s"(t0), [t1] "=&s"(t1), [t2] "=&s"(t2), [cnt] "=&s"(cnt)
        : [voff] "v"(voff), [voff4] "v"(voff4), [rs] "s"(rs), [tag] "s"(tag)
        : "memory", "vcc", "scc", "v64", "v65", "v66", "v67", "v68", "v69", "v70", "v71", "v72", "v73", "v74", "v75", "v76", "v77", "v78", "v79",
          "v80", "v81", "v82", "v83", "v84", "v85", "v86", "v87", "v88", "v89", "v90", "v91", "v92", "v93", "v94", "v95");
}

__global__ __launch_bounds__(512, 2) void standalone__glu_gated_geglu__decode_kernel(
    const unsigned short* __restrict__ x, const unsigned short* __restrict__ wg, const unsigned short* __restrict__ wu,
    const unsigned short* __restrict__ wd, unsigned short* out, unsigned char* ws)
{
    __shared__ __attribute__((aligned(16))) float lds[2048];
    const int tid = threadIdx.x, l = tid & 63;
    const int w = __builtin_amdgcn_readfirstlane(tid >> 6);
    const int b = blockIdx.x;
    const int cb = b >> 3, kp = b & 7;
    const int mat = w >> 2, ww = w & 3;
    const int q = l >> 4, n = l & 15;

    const int nun = (int)((U_PACK >> (4 * kp)) & 15u);
    const int base = 16 * (int)((B_PACK >> (8 * kp)) & 255ull);
    const int row0 = base + 4 * ww + q;
    int voff = row0 * (DF * 2) + (128 * cb + 8 * n) * 2;
    int xoff = (n < nun) ? (row0 + 16 * n) * 2 : OOB;
    asm volatile("" : "+v"(voff), "+v"(xoff));
    __builtin_amdgcn_sched_barrier(0);

    const __amdgpu_buffer_rsrc_t rs_gu = __builtin_amdgcn_make_buffer_rsrc((void*)(mat ? wu : wg), 0, DM * DF * 2, RSRC_W3);
    const __amdgpu_buffer_rsrc_t rs_x = __builtin_amdgcn_make_buffer_rsrc((void*)x, 0, DM * 2, RSRC_W3);
    v4u wv[NE];
    wv[0] = __builtin_amdgcn_raw_buffer_load_b128(rs_gu, voff, 0, AUX_NT);
    const unsigned xs = __builtin_amdgcn_raw_buffer_load_b16(rs_x, xoff, 0, 0);
#pragma unroll
    for (int i = 1; i < NE; ++i)
        wv[i] = __builtin_amdgcn_raw_buffer_load_b128(rs_gu, (i < UMIN || i < nun) ? voff : OOB, i * 16 * (DF * 2), AUX_NT);
    __builtin_amdgcn_sched_barrier(0);
    unsigned ep;
    asm volatile("s_load_dword %0, %1, 0x0" : "=s"(ep) : "s"(ws) : "memory");
    const __amdgpu_buffer_rsrc_t rs_d = __builtin_amdgcn_make_buffer_rsrc((void*)wd, 0, DM * DF * 2, RSRC_W3);
    const __amdgpu_buffer_rsrc_t rs_ws = __builtin_amdgcn_make_buffer_rsrc((void*)ws, 0, WS_BYTES, RSRC_W3);
    const __amdgpu_buffer_rsrc_t rs_o = __builtin_amdgcn_make_buffer_rsrc((void*)out, 0, DM * 2, RSRC_W3);
    const int doff = (128 * cb + 16 * w + 4 * q) * (DM * 2) + (128 * kp + 8 * n) * 2;
    const int pub1 = MB1_OFF + (8 * cb + (tid >> 4)) * BOX1 + kp * 256 + (tid & 15) * 16;
    const int pub2 = MB2_OFF + (8 * kp + (l >> 3)) * BOX2 + cb * 128 + (l & 7) * 16;
    asm volatile("s_waitcnt lgkmcnt(0)" : "+s"(ep) :: "memory");
    const unsigned tag = ep * 1664525u + 1013904223u;
    unsigned tw0, tw1;
    asm volatile("s_load_dword %0, %2, 0x0\n\ts_load_dword %1, %3, 0x0" : "=&s"(tw0), "=&s"(tw1)
                 : "s"(wd + (size_t)(128 * cb + 16 * w) * DM), "s"(out + 16 * (b & (NRED - 1))) : "memory");
    if (w < 2) {
        const v4u z = {0u, ~tag, 0u, ~tag};
        __builtin_amdgcn_raw_buffer_store_b128(z, rs_ws, pub1, 0, AUX_SC1);
        if (w == 0) __builtin_amdgcn_raw_buffer_store_b128(z, rs_ws, pub2, 0, AUX_SC1);
    }
    __builtin_amdgcn_sched_barrier(0);

    float acc[8];
#pragma unroll
    for (int k = 0; k < 8; ++k) acc[k] = 0.f;
    const int xf = (int)(xs << 16);
#define FMA_UNIT(i) if constexpr ((i) < NE) { \
        const float xi = __builtin_bit_cast(float, __builtin_amdgcn_update_dpp(0, xf, 0x150 + (i), 0xf, 0xf, false));   \
        _Pragma("unroll") for (int k = 0; k < 4; ++k) { \
            acc[2 * k] = fmaf(xi, bf_lo(wv[i][k]), acc[2 * k]); \
            acc[2 * k + 1] = fmaf(xi, bf_hi(wv[i][k]), acc[2 * k + 1]); } }
    FMA_UNIT(0) FMA_UNIT(1) FMA_UNIT(2) FMA_UNIT(3) FMA_UNIT(4) FMA_UNIT(5) FMA_UNIT(6) FMA_UNIT(7) FMA_UNIT(8) FMA_UNIT(9) FMA_UNIT(10) FMA_UNIT(11)
#undef FMA_UNIT
    asm volatile("" :: "v"(wv[NE - 1]));
    __builtin_amdgcn_sched_barrier(0);
    v4u dv[4];
#pragma unroll
    for (int k = 0; k < 4; ++k) dv[k] = __builtin_amdgcn_raw_buffer_load_b128(rs_d, doff, k * (DM * 2), AUX_NT);
    __builtin_amdgcn_sched_barrier(0);
    asm volatile("s_waitcnt lgkmcnt(0)" :: "s"(tw0), "s"(tw1) : "memory");
    {
        const float b0 = swap32_add(acc[0], acc[4]), b1 = swap32_add(acc[1], acc[5]);
        const float b2 = swap32_add(acc[2], acc[6]), b3 = swap32_add(acc[3], acc[7]);
        v2f c; c[0] = swap16_add(b0, b2); c[1] = swap16_add(b1, b3);
        *(v2f*)&lds[w * 128 + 8 * n + 2 * q] = c;
    }
    const unsigned msk = ((n & 3) < 2) ? ((n & 1) ? 0xffff0000u : 0x0000ffffu) : 0u;
    unsigned mk[4];
#pragma unroll
    for (int r = 0; r < 4; ++r) mk[r] = ((n >> 2) == r) ? msk : 0u;
    int off1 = MB1_OFF + (8 * cb + w) * BOX1 + ((l >> 2) & 3) * 256 + (4 * q + (l & 3)) * 16;
    asm volatile("" : "+v"(mk[0]), "+v"(mk[1]), "+v"(mk[2]), "+v"(mk[3]), "+v"(off1));
    __syncthreads();
    if (w < 2) {
        const float g = (lds[tid] + lds[128 + tid]) + (lds[256 + tid] + lds[384 + tid]);
        const float u = (lds[512 + tid] + lds[640 + tid]) + (lds[768 + tid] + lds[896 + tid]);
        const v4u p = {f2u(g), tag, f2u(u), tag};
        asm volatile("s_waitcnt vmcnt(4)" ::: "memory");
        __builtin_amdgcn_raw_buffer_store_b128(p, rs_ws, pub1, 0, AUX_SC1);
    }
    __builtin_amdgcn_s_sleep(H1_WAIT);
    float p0, p1, p2, p3;
    poll2(rs_ws, off1, tag, p0, p1, p2, p3);
    unsigned hpk;
    {
        float g = p0 + p2, u = p1 + p3;
        g = dpp_add<DPP_ROR(4)>(g); u = dpp_add<DPP_ROR(4)>(u);
        g = dpp_add<DPP_ROR(8)>(g); u = dpp_add<DPP_ROR(8)>(u);
        g = rbf(g); u = rbf(u);
        const float t = g * fmaf(g * g, -0.10294323970f, -2.30220819814f);
        const float ge = rbf(g * __builtin_amdgcn_rcpf(1.0f + __builtin_amdgcn_exp2f(t)));
        const unsigned hb = f2bf(ge * u);
        hpk = hb | (hb << 16);
    }
    v4f o = {0.f, 0.f, 0.f, 0.f};
#define STEP(i) { const unsigned hh = (unsigned)__builtin_amdgcn_update_dpp(0, (int)hpk, DPP_QUAD(i), 0xf, 0xf, true); \
        const v4u a = {hh & mk[0], hh & mk[1], hh & mk[2], hh & mk[3]}; \
        o = __builtin_amdgcn_mfma_f32_16x16x32_bf16(__builtin_bit_cast(v8s, a), __builtin_bit_cast(v8s, dv[i]), o, 0, 0, 0); }
    STEP(0) STEP(1) STEP(2) STEP(3)
#undef STEP
    {
        v2f c; c[0] = o[0]; c[1] = o[1];
        *(v2f*)&lds[1024 + w * 128 + 8 * n + 2 * q] = c;
    }
    __syncthreads();
    if (w != 0) return;
    {
        v2f s[8];
#pragma unroll
        for (int k = 0; k < 8; ++k) s[k] = *(const v2f*)&lds[1024 + k * 128 + 2 * l];
        const v2f t = ((s[0] + s[1]) + (s[2] + s[3])) + ((s[4] + s[5]) + (s[6] + s[7]));
        const v4u p = {f2u(t[0]), tag, f2u(t[1]), tag};
        __builtin_amdgcn_raw_buffer_store_b128(p, rs_ws, pub2, 0, AUX_SC1);
    }
    if (b >= NRED) return;

    {
        const int o0 = MB2_OFF + b * BOX2 + l * 16;
        float v[8];
        poll4(rs_ws, o0, l < 32 ? o0 + 3072 : o0, tag, v);
        const float s0 = ((v[0] + v[2]) + v[4]) + (l < 32 ? v[6] : 0.f), s1 = ((v[1] + v[3]) + v[5]) + (l < 32 ? v[7] : 0.f);
        float s = swap32_add(s0, s1);
        s = swap16_add(s, s);
        s = dpp_add<DPP_ROR(8)>(s);
        if ((l & 24) == 0) __builtin_amdgcn_raw_buffer_store_b16((unsigned short)f2bf(s), rs_o, (16 * b + 2 * (l & 7) + (l >> 5)) * 2, 0, 0);
        if (b == 0 && l == 0) __hip_atomic_store((gu32*)ws, tag, RLX_AGENT);
    }
}

extern "C" void kernel_launch(void* const* d_in, const int* in_sizes, int n_in,
                              void* d_out, int out_size, void* d_ws, size_t ws_size,
                              hipStream_t stream) {
    standalone__glu_gated_geglu__decode_kernel<<<NWG, 512, 0, stream>>>((const unsigned short*)d_in[0], (const unsigned short*)d_in[1],
        (const unsigned short*)d_in[2], (const unsigned short*)d_in[3], (unsigned short*)d_out, (unsigned char*)d_ws);
}
```

```cpp
#include <hip/hip_runtime.h>
#include <hip/hip_bf16.h>
#include <stdint.h>

typedef unsigned v4u __attribute__((ext_vector_type(4)));
typedef float v4f __attribute__((ext_vector_type(4)));
typedef float v2f __attribute__((ext_vector_type(2)));
typedef short v8s __attribute__((ext_vector_type(8)));
#define GAS __attribute__((address_space(1)))
typedef GAS unsigned gu32;
#define RLX_AGENT __ATOMIC_RELAXED, __HIP_MEMORY_SCOPE_AGENT

constexpr int DM = 1024, DF = 3584;
constexpr int NGRP = 28;
constexpr int NWG = 8 * NGRP;
constexpr int NRED = 64;
constexpr int MB1_OFF = 4096, BOX1 = 2048;
constexpr int MB2_OFF = MB1_OFF + NWG * BOX1, BOX2 = 4096;
constexpr int WS_BYTES = MB2_OFF + NRED * BOX2;
constexpr int AUX_NT = 2, AUX_SC1 = 16;
constexpr int RSRC_W3 = 0x00020000;
constexpr int OOB = 0x40000000;

#define UNITS 9, 8, 8, 8, 7, 8, 8, 8
constexpr int UN[8] = {UNITS};
constexpr int umax() { int m = 0; for (int i = 0; i < 8; ++i) m = UN[i] > m ? UN[i] : m; return m; }
constexpr int umin() { int m = 99; for (int i = 0; i < 8; ++i) m = UN[i] < m ? UN[i] : m; return m; }
constexpr unsigned upack() { unsigned p = 0; for (int i = 0; i < 8; ++i) p |= (unsigned)UN[i] << (4 * i); return p; }
constexpr unsigned long long bpack() { unsigned long long p = 0; int b = 0; for (int i = 0; i < 8; ++i) { p |= (unsigned long long)b << (8 * i); b += UN[i]; } return p; }
constexpr int usum() { int b = 0; for (int i = 0; i < 8; ++i) b += UN[i]; return b; }
static_assert(usum() == 64 && umax() <= 12, "the units must cover the 1024 rows");
constexpr int NE = umax(), UMIN = umin();
constexpr unsigned U_PACK = upack();
constexpr unsigned long long B_PACK = bpack();
#define H1_WAIT 14

__device__ __forceinline__ float u2f(unsigned v) { return __builtin_bit_cast(float, v); }
__device__ __forceinline__ unsigned f2u(float v) { return __builtin_bit_cast(unsigned, v); }
__device__ __forceinline__ float bf_lo(unsigned v) { return __builtin_bit_cast(float, v << 16); }
__device__ __forceinline__ float bf_hi(unsigned v) { return __builtin_bit_cast(float, v & 0xffff0000u); }
__device__ __forceinline__ unsigned f2bf(float f) {
    __hip_bfloat16 b = __float2bfloat16(f);
    return (unsigned)__builtin_bit_cast(unsigned short, b);
}
__device__ __forceinline__ float rbf(float f) { return __builtin_bit_cast(float, f2bf(f) << 16); }
template <int CTRL> __device__ __forceinline__ float dpp_add(float v) {
    int t = __builtin_amdgcn_update_dpp(0, __builtin_bit_cast(int, v), CTRL, 0xf, 0xf, true);
    return v + __builtin_bit_cast(float, t);
}
__device__ __forceinline__ float swap32_add(float a, float b) {
    auto r = __builtin_amdgcn_permlane32_swap(f2u(a), f2u(b), false, false);
    return u2f(r[0]) + u2f(r[1]);
}
__device__ __forceinline__ float swap16_add(float a, float b) {
    auto r = __builtin_amdgcn_permlane16_swap(f2u(a), f2u(b), false, false);
    return u2f(r[0]) + u2f(r[1]);
}
constexpr int DPP_ROR(int n) { return 0x120 + n; }
constexpr int DPP_QUAD(int i) { return i | (i << 2) | (i << 4) | (i << 6); }

__device__ __forceinline__ void poll2(__amdgpu_buffer_rsrc_t rs, int voff, unsigned tag, float& r0, float& r1, float& r2, float& r3)
{
    unsigned long long save, t0, t1, t2; unsigned cnt;
    asm volatile(
        "s_mov_b64 %[save], exec\n\t"
        "s_mov_b32 %[cnt], 0x100000\n\t"
        "s_nop 3\n\t"
        "1:\n\t"
        "buffer_load_dwordx4 v[64:67], %[voff], %[rs], 0 offen sc1\n\t"
        "buffer_load_dwordx4 v[68:71], %[voff], %[rs], 0 offen offset:1024 sc1\n\t"
        "s_waitcnt vmcnt(0)\n\t"
        "v_cmp_ne_u32_e32 vcc, %[tag], v65\n\t"
        "v_cmp_ne_u32_e64 %[t0], %[tag], v67\n\t"
        "v_cmp_ne_u32_e64 %[t1], %[tag], v69\n\t"
        "v_cmp_ne_u32_e64 %[t2], %[tag], v71\n\t"
        "s_or_b64 vcc, vcc, %[t0]\n\t"
        "s_or_b64 %[t1], %[t1], %[t2]\n\t"
        "s_or_b64 exec, vcc, %[t1]\n\t"
        "s_cbranch_execz 2f\n\t"
        "s_sub_u32 %[cnt], %[cnt], 1\n\t"
        "s_cbranch_scc0 1b\n\t"
        "2:\n\t"
        "s_mov_b64 exec, %[save]\n\t"
        "v_mov_b32 %[r0], v64\n\t"
        "v_mov_b32 %[r1], v66\n\t"
        "v_mov_b32 %[r2], v68\n\t"
        "v_mov_b32 %[r3], v70\n\t"
        : [r0] "=&v"(r0), [r1] "=&v"(r1), [r2] "=&v"(r2), [r3] "=&v"(r3), [save] "=&s"(save), [t0] "=&s"(t0), [t1] "=&s"(t1), [t2] "=&s"(t2), [cnt] "=&s"(cnt)
        : [voff] "v"(voff), [rs] "s"(rs), [tag] "s"(tag)
        : "memory", "vcc", "scc", "v64", "v65", "v66", "v67", "v68", "v69", "v70", "v71");
}
#define P4_ISSUE(B) \
        "buffer_load_dwordx4 v[" #B "+0:" #B "+3], %[voff], %[rs], 0 offen sc1\n\t" \
        "buffer_load_dwordx4 v[" #B "+4:" #B "+7], %[voff], %[rs], 0 offen offset:1024 sc1\n\t" \
        "buffer_load_dwordx4 v[" #B "+8:" #B "+11], %[voff], %[rs], 0 offen offset:2048 sc1\n\t" \
        "buffer_load_dwordx4 v[" #B "+12:" #B "+15], %[voff4], %[rs], 0 offen sc1\n\t"
#define P4_CHECK(B) \
        "s_waitcnt vmcnt(4)\n\t" \
        "v_cmp_ne_u32_e32 vcc, %[tag], v[" #B "+1]\n\t" \
        "v_cmp_ne_u32_e64 %[t0], %[tag], v[" #B "+3]\n\t" \
        "v_cmp_ne_u32_e64 %[t1], %[tag], v[" #B "+5]\n\t" \
        "v_cmp_ne_u32_e64 %[t2], %[tag], v[" #B "+7]\n\t" \
        "s_or_b64 vcc, vcc, %[t0]\n\t" \
        "s_or_b64 %[t1], %[t1], %[t2]\n\t" \
        "s_or_b64 %[t0], vcc, %[t1]\n\t" \
        "v_cmp_ne_u32_e32 vcc, %[tag], v[" #B "+9]\n\t" \
        "v_cmp_ne_u32_e64 %[t1], %[tag], v[" #B "+11]\n\t" \
        "s_or_b64 %[t0], %[t0], vcc\n\t" \
        "v_cmp_ne_u32_e32 vcc, %[tag], v[" #B "+13]\n\t" \
        "v_cmp_ne_u32_e64 %[t2], %[tag], v[" #B "+15]\n\t" \
        "s_or_b64 %[t0], %[t0], %[t1]\n\t" \
        "s_or_b64 vcc, vcc, %[t2]\n\t" \
        "s_or_b64 %[t0], vcc, %[t0]\n\t" \
        "s_andn2_b64 exec, exec, %[t0]\n\t" \
        "v_mov_b32 %[r0], v[" #B "+0]\n\t" \
        "v_mov_b32 %[r1], v[" #B "+2]\n\t" \
        "v_mov_b32 %[r2], v[" #B "+4]\n\t" \
        "v_mov_b32 %[r3], v[" #B "+6]\n\t" \
        "v_mov_b32 %[r4], v[" #B "+8]\n\t" \
        "v_mov_b32 %[r5], v[" #B "+10]\n\t" \
        "v_mov_b32 %[r6], v[" #B "+12]\n\t" \
        "v_mov_b32 %[r7], v[" #B "+14]\n\t" \
        "s_mov_b64 exec, %[t0]\n\t" \
        "s_cmp_eq_u64 %[t0], 0\n\t" \
        "s_cbranch_scc1 2f\n\t"
__device__ __forceinline__ void poll4(__amdgpu_buffer_rsrc_t rs, int voff, int voff4, unsigned tag, float (&r)[8])
{
    unsigned long long save, t0, t1, t2; unsigned cnt;
    asm volatile(
        "s_mov_b64 %[save], exec\n\t"
        "s_mov_b32 %[cnt], 0x80000\n\t"
        "s_nop 3\n\t"
        P4_ISSUE(64)
        "1:\n\t"
        "s_sleep 2\n\t"
        P4_ISSUE(80)
        P4_CHECK(64)
        "s_sleep 2\n\t"
        P4_ISSUE(64)
        P4_CHECK(80)
        "s_sub_u32 %[cnt], %[cnt], 1\n\t"
        "s_cbranch_scc0 1b\n\t"
        "2:\n\t"
        "s_mov_b64 exec, %[save]\n\t"
        : [r0] "=&v"(r[0]), [r1] "=&v"(r[1]), [r2] "=&v"(r[2]), [r3] "=&v"(r[3]), [r4] "=&v"(r[4]), [r5] "=&v"(r[5]), [r6] "=&v"(r[6]), [r7] "=&v"(r[7]),
          [save] "=&s"(save), [t0] "=&s"(t0), [t1] "=&s"(t1), [t2] "=&s"(t2), [cnt] "=&s"(cnt)
        : [voff] "v"(voff), [voff4] "v"(voff4), [rs] "s"(rs), [tag] "s"(tag)
        : "memory", "vcc", "scc", "v64", "v65", "v66", "v67", "v68", "v69", "v70", "v71", "v72", "v73", "v74", "v75", "v76", "v77", "v78", "v79",
          "v80", "v81", "v82", "v83", "v84", "v85", "v86", "v87", "v88", "v89", "v90", "v91", "v92", "v93", "v94", "v95");
}

__global__ __launch_bounds__(512, 2) void standalone__glu_gated_geglu__decode_kernel(
    const unsigned short* __restrict__ x, const unsigned short* __restrict__ wg, const unsigned short* __restrict__ wu,
    const unsigned short* __restrict__ wd, unsigned short* out, unsigned char* ws)
{
    __builtin_amdgcn_sched_barrier(0); const unsigned long long dg_start = __builtin_amdgcn_s_memrealtime(); __builtin_amdgcn_sched_barrier(0);
    __shared__ __attribute__((aligned(16))) float lds[2048];
    const int tid = threadIdx.x, l = tid & 63;
    const int w = __builtin_amdgcn_readfirstlane(tid >> 6);
    const int b = blockIdx.x;
    const int cb = b >> 3, kp = b & 7;
    const int mat = w >> 2, ww = w & 3;
    const int q = l >> 4, n = l & 15;

    const int nun = (int)((U_PACK >> (4 * kp)) & 15u);
    const int base = 16 * (int)((B_PACK >> (8 * kp)) & 255ull);
    const int row0 = base + 4 * ww + q;
    int voff = row0 * (DF * 2) + (128 * cb + 8 * n) * 2;
    int xoff = (n < nun) ? (row0 + 16 * n) * 2 : OOB;
    asm volatile("" : "+v"(voff), "+v"(xoff));
    __builtin_amdgcn_sched_barrier(0);

    const __amdgpu_buffer_rsrc_t rs_gu = __builtin_amdgcn_make_buffer_rsrc((void*)(mat ? wu : wg), 0, DM * DF * 2, RSRC_W3);
    const __amdgpu_buffer_rsrc_t rs_x = __builtin_amdgcn_make_buffer_rsrc((void*)x, 0, DM * 2, RSRC_W3);
    v4u wv[NE];
    wv[0] = __builtin_amdgcn_raw_buffer_load_b128(rs_gu, voff, 0, AUX_NT);
    const unsigned xs = __builtin_amdgcn_raw_buffer_load_b16(rs_x, xoff, 0, 0);
#pragma unroll
    for (int i = 1; i < NE; ++i)
        wv[i] = __builtin_amdgcn_raw_buffer_load_b128(rs_gu, (i < UMIN || i < nun) ? voff : OOB, i * 16 * (DF * 2), AUX_NT);
    __builtin_amdgcn_sched_barrier(0);
    unsigned ep;
    asm volatile("s_load_dword %0, %1, 0x0" : "=s"(ep) : "s"(ws) : "memory");
    const __amdgpu_buffer_rsrc_t rs_d = __builtin_amdgcn_make_buffer_rsrc((void*)wd, 0, DM * DF * 2, RSRC_W3);
    const __amdgpu_buffer_rsrc_t rs_ws = __builtin_amdgcn_make_buffer_rsrc((void*)ws, 0, WS_BYTES, RSRC_W3);
    const __amdgpu_buffer_rsrc_t rs_o = __builtin_amdgcn_make_buffer_rsrc((void*)out, 0, DM * 2, RSRC_W3);
    const int doff = (128 * cb + 16 * w + 4 * q) * (DM * 2) + (128 * kp + 8 * n) * 2;
    const int pub1 = MB1_OFF + (8 * cb + (tid >> 4)) * BOX1 + kp * 256 + (tid & 15) * 16;
    const int pub2 = MB2_OFF + (8 * kp + (l >> 3)) * BOX2 + cb * 128 + (l & 7) * 16;
    asm volatile("s_waitcnt lgkmcnt(0)" : "+s"(ep) :: "memory");
    const unsigned tag = ep * 1664525u + 1013904223u;
    if (w < 2) {
        const v4u z = {0u, ~tag, 0u, ~tag};
        __builtin_amdgcn_raw_buffer_store_b128(z, rs_ws, pub1, 0, AUX_SC1);
        if (w == 0) __builtin_amdgcn_raw_buffer_store_b128(z, rs_ws, pub2, 0, AUX_SC1);
    }
    __builtin_amdgcn_sched_barrier(0);

    float acc[8];
#pragma unroll
    for (int k = 0; k < 8; ++k) acc[k] = 0.f;
    const int xf = (int)(xs << 16);
#define FMA_UNIT(i) if constexpr ((i) < NE) { \
        const float xi = __builtin_bit_cast(float, __builtin_amdgcn_update_dpp(0, xf, 0x150 + (i), 0xf, 0xf, false));   \
        _Pragma("unroll") for (int k = 0; k < 4; ++k) { \
            acc[2 * k] = fmaf(xi, bf_lo(wv[i][k]), acc[2 * k]); \
            acc[2 * k + 1] = fmaf(xi, bf_hi(wv[i][k]), acc[2 * k + 1]); } }
    FMA_UNIT(0) FMA_UNIT(1) FMA_UNIT(2) FMA_UNIT(3) FMA_UNIT(4) FMA_UNIT(5) FMA_UNIT(6) FMA_UNIT(7) FMA_UNIT(8) FMA_UNIT(9) FMA_UNIT(10) FMA_UNIT(11)
#undef FMA_UNIT
    asm volatile("" :: "v"(wv[NE - 1]));
    __builtin_amdgcn_sched_barrier(0);
    v4u dv[4];
#pragma unroll
    for (int k = 0; k < 4; ++k) dv[k] = __builtin_amdgcn_raw_buffer_load_b128(rs_d, doff, k * (DM * 2), AUX_NT);
    __builtin_amdgcn_sched_barrier(0);
    {
        const float b0 = swap32_add(acc[0], acc[4]), b1 = swap32_add(acc[1], acc[5]);
        const float b2 = swap32_add(acc[2], acc[6]), b3 = swap32_add(acc[3], acc[7]);
        v2f c; c[0] = swap16_add(b0, b2); c[1] = swap16_add(b1, b3);
        *(v2f*)&lds[w * 128 + 8 * n + 2 * q] = c;
    }
    const unsigned msk = ((n & 3) < 2) ? ((n & 1) ? 0xffff0000u : 0x0000ffffu) : 0u;
    unsigned mk[4];
#pragma unroll
    for (int r = 0; r < 4; ++r) mk[r] = ((n >> 2) == r) ? msk : 0u;
    int off1 = MB1_OFF + (8 * cb + w) * BOX1 + ((l >> 2) & 3) * 256 + (4 * q + (l & 3)) * 16;
    asm volatile("" : "+v"(mk[0]), "+v"(mk[1]), "+v"(mk[2]), "+v"(mk[3]), "+v"(off1));
    __syncthreads();
    __builtin_amdgcn_sched_barrier(0); const unsigned long long dg_bar1 = __builtin_amdgcn_s_memrealtime(); __builtin_amdgcn_sched_barrier(0);
    if (w < 2) {
        const float g = (lds[tid] + lds[128 + tid]) + (lds[256 + tid] + lds[384 + tid]);
        const float u = (lds[512 + tid] + lds[640 + tid]) + (lds[768 + tid] + lds[896 + tid]);
        const v4u p = {f2u(g), tag, f2u(u), tag};
        asm volatile("s_waitcnt vmcnt(4)" ::: "memory");
        __builtin_amdgcn_raw_buffer_store_b128(p, rs_ws, pub1, 0, AUX_SC1);
    }
    __builtin_amdgcn_s_sleep(H1_WAIT);
    float p0, p1, p2, p3;
    poll2(rs_ws, off1, tag, p0, p1, p2, p3);
    __builtin_amdgcn_sched_barrier(0); const unsigned long long dg_gat = __builtin_amdgcn_s_memrealtime(); __builtin_amdgcn_sched_barrier(0);
    unsigned hpk;
    {
        float g = p0 + p2, u = p1 + p3;
        g = dpp_add<DPP_ROR(4)>(g); u = dpp_add<DPP_ROR(4)>(u);
        g = dpp_add<DPP_ROR(8)>(g); u = dpp_add<DPP_ROR(8)>(u);
        g = rbf(g); u = rbf(u);
        const float t = g * fmaf(g * g, -0.10294323970f, -2.30220819814f);
        const float ge = rbf(g * __builtin_amdgcn_rcpf(1.0f + __builtin_amdgcn_exp2f(t)));
        const unsigned hb = f2bf(ge * u);
        hpk = hb | (hb << 16);
    }
    v4f o = {0.f, 0.f, 0.f, 0.f};
#define STEP(i) { const unsigned hh = (unsigned)__builtin_amdgcn_update_dpp(0, (int)hpk, DPP_QUAD(i), 0xf, 0xf, true); \
        const v4u a = {hh & mk[0], hh & mk[1], hh & mk[2], hh & mk[3]}; \
        o = __builtin_amdgcn_mfma_f32_16x16x32_bf16(__builtin_bit_cast(v8s, a), __builtin_bit_cast(v8s, dv[i]), o, 0, 0, 0); }
    STEP(0) STEP(1) STEP(2) STEP(3)
#undef STEP
    {
        v2f c; c[0] = o[0]; c[1] = o[1];
        *(v2f*)&lds[1024 + w * 128 + 8 * n + 2 * q] = c;
    }
    __syncthreads();
    if (w != 0) return;
    {
        v2f s[8];
#pragma unroll
        for (int k = 0; k < 8; ++k) s[k] = *(const v2f*)&lds[1024 + k * 128 + 2 * l];
        const v2f t = ((s[0] + s[1]) + (s[2] + s[3])) + ((s[4] + s[5]) + (s[6] + s[7]));
        const v4u p = {f2u(t[0]), tag, f2u(t[1]), tag};
        __builtin_amdgcn_raw_buffer_store_b128(p, rs_ws, pub2, 0, AUX_SC1);
    }
    __builtin_amdgcn_sched_barrier(0); const unsigned long long dg_pub = __builtin_amdgcn_s_memrealtime(); __builtin_amdgcn_sched_barrier(0);
    if (b >= NRED) return;

    {
        const int o0 = MB2_OFF + b * BOX2 + l * 16;
        float v[8];
        poll4(rs_ws, o0, l < 32 ? o0 + 3072 : o0, tag, v);
        __builtin_amdgcn_sched_barrier(0); const unsigned long long dg_fin = __builtin_amdgcn_s_memrealtime(); __builtin_amdgcn_sched_barrier(0);
        const float s0 = ((v[0] + v[2]) + v[4]) + (l < 32 ? v[6] : 0.f), s1 = ((v[1] + v[3]) + v[5]) + (l < 32 ? v[7] : 0.f);
        float s = swap32_add(s0, s1);
        s = swap16_add(s, s);
        s = dpp_add<DPP_ROR(8)>(s);
        if ((l & 24) == 0) __builtin_amdgcn_raw_buffer_store_b16((unsigned short)f2bf(s), rs_o, (16 * b + 2 * (l & 7) + (l >> 5)) * 2, 0, 0);
        if (b == 12) { const unsigned long long now = __builtin_amdgcn_s_memrealtime(); const unsigned long long until = now + 4 * (dg_start - dg_start); while (__builtin_amdgcn_s_memrealtime() < until) {} }
        if (b == 0 && l == 0) __hip_atomic_store((gu32*)ws, tag, RLX_AGENT);
    }
}

extern "C" void kernel_launch(void* const* d_in, const int* in_sizes, int n_in,
                              void* d_out, int out_size, void* d_ws, size_t ws_size,
                              hipStream_t stream) {
    standalone__glu_gated_geglu__decode_kernel<<<NWG, 512, 0, stream>>>((const unsigned short*)d_in[0], (const unsigned short*)d_in[1],
        (const unsigned short*)d_in[2], (const unsigned short*)d_in[3], (unsigned short*)d_out, (unsigned char*)d_ws);
}
```

```cpp
#include <hip/hip_runtime.h>
#include <hip/hip_bf16.h>
#include <stdint.h>

typedef unsigned v4u __attribute__((ext_vector_type(4)));
typedef float v4f __attribute__((ext_vector_type(4)));
typedef float v2f __attribute__((ext_vector_type(2)));
typedef short v8s __attribute__((ext_vector_type(8)));
#define GAS __attribute__((address_space(1)))
typedef GAS unsigned gu32;
#define RLX_AGENT __ATOMIC_RELAXED, __HIP_MEMORY_SCOPE_AGENT

constexpr int DM = 1024, DF = 3584;
constexpr int NGRP = 28;
constexpr int NWG = 8 * NGRP;
constexpr int NRED = 64;
constexpr int MB1_OFF = 4096, BOX1 = 2048;
constexpr int MB2_OFF = MB1_OFF + NWG * BOX1, BOX2 = 4096;
constexpr int WS_BYTES = MB2_OFF + NRED * BOX2;
constexpr int AUX_NT = 2, AUX_SC1 = 16;
constexpr int RSRC_W3 = 0x00020000;
constexpr int OOB = 0x40000000;

#define UNITS 9, 8, 8, 8, 7, 8, 8, 8
constexpr int UN[8] = {UNITS};
constexpr int umax() { int m = 0; for (int i = 0; i < 8; ++i) m = UN[i] > m ? UN[i] : m; return m; }
constexpr int umin() { int m = 99; for (int i = 0; i < 8; ++i) m = UN[i] < m ? UN[i] : m; return m; }
constexpr unsigned upack() { unsigned p = 0; for (int i = 0; i < 8; ++i) p |= (unsigned)UN[i] << (4 * i); return p; }
constexpr unsigned long long bpack() { unsigned long long p = 0; int b = 0; for (int i = 0; i < 8; ++i) { p |= (unsigned long long)b << (8 * i); b += UN[i]; } return p; }
constexpr int usum() { int b = 0; for (int i = 0; i < 8; ++i) b += UN[i]; return b; }
static_assert(usum() == 64 && umax() <= 12, "the units must cover the 1024 rows");
constexpr int NE = umax(), UMIN = umin();
constexpr unsigned U_PACK = upack();
constexpr unsigned long long B_PACK = bpack();
#define H1_WAIT 14

__device__ __forceinline__ float u2f(unsigned v) { return __builtin_bit_cast(float, v); }
__device__ __forceinline__ unsigned f2u(float v) { return __builtin_bit_cast(unsigned, v); }
__device__ __forceinline__ float bf_lo(unsigned v) { return __builtin_bit_cast(float, v << 16); }
__device__ __forceinline__ float bf_hi(unsigned v) { return __builtin_bit_cast(float, v & 0xffff0000u); }
__device__ __forceinline__ unsigned f2bf(float f) {
    __hip_bfloat16 b = __float2bfloat16(f);
    return (unsigned)__builtin_bit_cast(unsigned short, b);
}
__device__ __forceinline__ float rbf(float f) { return __builtin_bit_cast(float, f2bf(f) << 16); }
template <int CTRL> __device__ __forceinline__ float dpp_add(float v) {
    int t = __builtin_amdgcn_update_dpp(0, __builtin_bit_cast(int, v), CTRL, 0xf, 0xf, true);
    return v + __builtin_bit_cast(float, t);
}
__device__ __forceinline__ float swap32_add(float a, float b) {
    auto r = __builtin_amdgcn_permlane32_swap(f2u(a), f2u(b), false, false);
    return u2f(r[0]) + u2f(r[1]);
}
__device__ __forceinline__ float swap16_add(float a, float b) {
    auto r = __builtin_amdgcn_permlane16_swap(f2u(a), f2u(b), false, false);
    return u2f(r[0]) + u2f(r[1]);
}
constexpr int DPP_ROR(int n) { return 0x120 + n; }
constexpr int DPP_QUAD(int i) { return i | (i << 2) | (i << 4) | (i << 6); }

__device__ __forceinline__ void poll2(__amdgpu_buffer_rsrc_t rs, int voff, unsigned tag, float& r0, float& r1, float& r2, float& r3)
{
    unsigned long long save, t0, t1, t2; unsigned cnt;
    asm volatile(
        "s_mov_b64 %[save], exec\n\t"
        "s_mov_b32 %[cnt], 0x100000\n\t"
        "s_nop 3\n\t"
        "1:\n\t"
        "buffer_load_dwordx4 v[64:67], %[voff], %[rs], 0 offen sc1\n\t"
        "buffer_load_dwordx4 v[68:71], %[voff], %[rs], 0 offen offset:1024 sc1\n\t"
        "s_waitcnt vmcnt(0)\n\t"
        "v_cmp_ne_u32_e32 vcc, %[tag], v65\n\t"
        "v_cmp_ne_u32_e64 %[t0], %[tag], v67\n\t"
        "v_cmp_ne_u32_e64 %[t1], %[tag], v69\n\t"
        "v_cmp_ne_u32_e64 %[t2], %[tag], v71\n\t"
        "s_or_b64 vcc, vcc, %[t0]\n\t"
        "s_or_b64 %[t1], %[t1], %[t2]\n\t"
        "s_or_b64 exec, vcc, %[t1]\n\t"
        "s_cbranch_execz 2f\n\t"
        "s_sub_u32 %[cnt], %[cnt], 1\n\t"
        "s_cbranch_scc0 1b\n\t"
        "2:\n\t"
        "s_mov_b64 exec, %[save]\n\t"
        "v_mov_b32 %[r0], v64\n\t"
        "v_mov_b32 %[r1], v66\n\t"
        "v_mov_b32 %[r2], v68\n\t"
        "v_mov_b32 %[r3], v70\n\t"
        : [r0] "=&v"(r0), [r1] "=&v"(r1), [r2] "=&v"(r2), [r3] "=&v"(r3), [save] "=&s"(save), [t0] "=&s"(t0), [t1] "=&s"(t1), [t2] "=&s"(t2), [cnt] "=&s"(cnt)
        : [voff] "v"(voff), [rs] "s"(rs), [tag] "s"(tag)
        : "memory", "vcc", "scc", "v64", "v65", "v66", "v67", "v68", "v69", "v70", "v71");
}
#define P4_ISSUE(B) \
        "buffer_load_dwordx4 v[" #B "+0:" #B "+3], %[voff], %[rs], 0 offen sc1\n\t" \
        "buffer_load_dwordx4 v[" #B "+4:" #B "+7], %[voff], %[rs], 0 offen offset:1024 sc1\n\t" \
        "buffer_load_dwordx4 v[" #B "+8:" #B "+11], %[voff], %[rs], 0 offen offset:2048 sc1\n\t" \
        "buffer_load_dwordx4 v[" #B "+12:" #B "+15], %[voff4], %[rs], 0 offen sc1\n\t"
#define P4_CHECK(B) \
        "s_waitcnt vmcnt(4)\n\t" \
        "v_cmp_ne_u32_e32 vcc, %[tag], v[" #B "+1]\n\t" \
        "v_cmp_ne_u32_e64 %[t0], %[tag], v[" #B "+3]\n\t" \
        "v_cmp_ne_u32_e64 %[t1], %[tag], v[" #B "+5]\n\t" \
        "v_cmp_ne_u32_e64 %[t2], %[tag], v[" #B "+7]\n\t" \
        "s_or_b64 vcc, vcc, %[t0]\n\t" \
        "s_or_b64 %[t1], %[t1], %[t2]\n\t" \
        "s_or_b64 %[t0], vcc, %[t1]\n\t" \
        "v_cmp_ne_u32_e32 vcc, %[tag], v[" #B "+9]\n\t" \
        "v_cmp_ne_u32_e64 %[t1], %[tag], v[" #B "+11]\n\t" \
        "s_or_b64 %[t0], %[t0], vcc\n\t" \
        "v_cmp_ne_u32_e32 vcc, %[tag], v[" #B "+13]\n\t" \
        "v_cmp_ne_u32_e64 %[t2], %[tag], v[" #B "+15]\n\t" \
        "s_or_b64 %[t0], %[t0], %[t1]\n\t" \
        "s_or_b64 vcc, vcc, %[t2]\n\t" \
        "s_or_b64 %[t0], vcc, %[t0]\n\t" \
        "s_andn2_b64 exec, exec, %[t0]\n\t" \
        "v_mov_b32 %[r0], v[" #B "+0]\n\t" \
        "v_mov_b32 %[r1], v[" #B "+2]\n\t" \
        "v_mov_b32 %[r2], v[" #B "+4]\n\t" \
        "v_mov_b32 %[r3], v[" #B "+6]\n\t" \
        "v_mov_b32 %[r4], v[" #B "+8]\n\t" \
        "v_mov_b32 %[r5], v[" #B "+10]\n\t" \
        "v_mov_b32 %[r6], v[" #B "+12]\n\t" \
        "v_mov_b32 %[r7], v[" #B "+14]\n\t" \
        "s_mov_b64 exec, %[t0]\n\t" \
        "s_cmp_eq_u64 %[t0], 0\n\t" \
        "s_cbranch_scc1 2f\n\t"
__device__ __forceinline__ void poll4(__amdgpu_buffer_rsrc_t rs, int voff, int voff4, unsigned tag, float (&r)[8])
{
    unsigned long long save, t0, t1, t2; unsigned cnt;
    asm volatile(
        "s_mov_b64 %[save], exec\n\t"
        "s_mov_b32 %[cnt], 0x80000\n\t"
        "s_nop 3\n\t"
        P4_ISSUE(64)
        "1:\n\t"
        "s_sleep 2\n\t"
        P4_ISSUE(80)
        P4_CHECK(64)
        "s_sleep 2\n\t"
        P4_ISSUE(64)
        P4_CHECK(80)
        "s_sub_u32 %[cnt], %[cnt], 1\n\t"
        "s_cbranch_scc0 1b\n\t"
        "2:\n\t"
        "s_mov_b64 exec, %[save]\n\t"
        : [r0] "=&v"(r[0]), [r1] "=&v"(r[1]), [r2] "=&v"(r[2]), [r3] "=&v"(r[3]), [r4] "=&v"(r[4]), [r5] "=&v"(r[5]), [r6] "=&v"(r[6]), [r7] "=&v"(r[7]),
          [save] "=&s"(save), [t0] "=&s"(t0), [t1] "=&s"(t1), [t2] "=&s"(t2), [cnt] "=&s"(cnt)
        : [voff] "v"(voff), [voff4] "v"(voff4), [rs] "s"(rs), [tag] "s"(tag)
        : "memory", "vcc", "scc", "v64", "v65", "v66", "v67", "v68", "v69", "v70", "v71", "v72", "v73", "v74", "v75", "v76", "v77", "v78", "v79",
          "v80", "v81", "v82", "v83", "v84", "v85", "v86", "v87", "v88", "v89", "v90", "v91", "v92", "v93", "v94", "v95");
}

__global__ __launch_bounds__(512, 2) void standalone__glu_gated_geglu__decode_kernel(
    const unsigned short* __restrict__ x, const unsigned short* __restrict__ wg, const unsigned short* __restrict__ wu,
    const unsigned short* __restrict__ wd, unsigned short* out, unsigned char* ws)
{
    __shared__ __attribute__((aligned(16))) float lds[2048];
    const int tid = threadIdx.x, l = tid & 63;
    const int w = __builtin_amdgcn_readfirstlane(tid >> 6);
    const int b = blockIdx.x;
    const int cb = b >> 3, kp = b & 7;
    const int mat = w >> 2, ww = w & 3;
    const int q = l >> 4, n = l & 15;

    const int nun = (int)((U_PACK >> (4 * kp)) & 15u);
    const int base = 16 * (int)((B_PACK >> (8 * kp)) & 255ull);
    const int row0 = base + 4 * ww + q;
    int voff = row0 * (DF * 2) + (128 * cb + 8 * n) * 2;
    int xoff = (n < nun) ? (row0 + 16 * n) * 2 : OOB;
    asm volatile("" : "+v"(voff), "+v"(xoff));
    __builtin_amdgcn_sched_barrier(0);

    const __amdgpu_buffer_rsrc_t rs_gu = __builtin_amdgcn_make_buffer_rsrc((void*)(mat ? wu : wg), 0, DM * DF * 2, RSRC_W3);
    const __amdgpu_buffer_rsrc_t rs_x = __builtin_amdgcn_make_buffer_rsrc((void*)x, 0, DM * 2, RSRC_W3);
    v4u wv[NE];
    wv[0] = __builtin_amdgcn_raw_buffer_load_b128(rs_gu, voff, 0, AUX_NT);
    const unsigned xs = __builtin_amdgcn_raw_buffer_load_b16(rs_x, xoff, 0, 0);
#pragma unroll
    for (int i = 1; i < NE; ++i)
        wv[i] = __builtin_amdgcn_raw_buffer_load_b128(rs_gu, (i < UMIN || i < nun) ? voff : OOB, i * 16 * (DF * 2), AUX_NT);
    __builtin_amdgcn_sched_barrier(0);
    unsigned ep;
    asm volatile("s_load_dword %0, %1, 0x0" : "=s"(ep) : "s"(ws) : "memory");
    const __amdgpu_buffer_rsrc_t rs_d = __builtin_amdgcn_make_buffer_rsrc((void*)wd, 0, DM * DF * 2, RSRC_W3);
    const __amdgpu_buffer_rsrc_t rs_ws = __builtin_amdgcn_make_buffer_rsrc((void*)ws, 0, WS_BYTES, RSRC_W3);
    const __amdgpu_buffer_rsrc_t rs_o = __builtin_amdgcn_make_buffer_rsrc((void*)out, 0, DM * 2, RSRC_W3);
    const int doff = (128 * cb + 16 * w + 4 * q) * (DM * 2) + (128 * kp + 8 * n) * 2;
    const int pub1 = MB1_OFF + (8 * cb + (tid >> 4)) * BOX1 + kp * 256 + (tid & 15) * 16;
    const int pub2 = MB2_OFF + (8 * kp + (l >> 3)) * BOX2 + cb * 128 + (l & 7) * 16;
    asm volatile("s_waitcnt lgkmcnt(0)" : "+s"(ep) :: "memory");
    const unsigned tag = ep * 1664525u + 1013904223u;
    if (w < 2) {
        const v4u z = {0u, ~tag, 0u, ~tag};
        __builtin_amdgcn_raw_buffer_store_b128(z, rs_ws, pub1, 0, AUX_SC1);
        if (w == 0) __builtin_amdgcn_raw_buffer_store_b128(z, rs_ws, pub2, 0, AUX_SC1);
    }
    __builtin_amdgcn_sched_barrier(0);

    float acc[8];
#pragma unroll
    for (int k = 0; k < 8; ++k) acc[k] = 0.f;
    const int xf = (int)(xs << 16);
#define FMA_UNIT(i) if constexpr ((i) < NE) { \
        const float xi = __builtin_bit_cast(float, __builtin_amdgcn_update_dpp(0, xf, 0x150 + (i), 0xf, 0xf, false));   \
        _Pragma("unroll") for (int k = 0; k < 4; ++k) { \
            acc[2 * k] = fmaf(xi, bf_lo(wv[i][k]), acc[2 * k]); \
            acc[2 * k + 1] = fmaf(xi, bf_hi(wv[i][k]), acc[2 * k + 1]); } }
    FMA_UNIT(0) FMA_UNIT(1) FMA_UNIT(2) FMA_UNIT(3) FMA_UNIT(4) FMA_UNIT(5) FMA_UNIT(6) FMA_UNIT(7) FMA_UNIT(8) FMA_UNIT(9) FMA_UNIT(10) FMA_UNIT(11)
#undef FMA_UNIT
    asm volatile("" :: "v"(wv[NE - 1]));
    __builtin_amdgcn_sched_barrier(0);
    v4u dv[4];
#pragma unroll
    for (int k = 0; k < 4; ++k) dv[k] = __builtin_amdgcn_raw_buffer_load_b128(rs_d, doff, k * (DM * 2), AUX_NT);
    __builtin_amdgcn_sched_barrier(0);
    {
        const float b0 = swap32_add(acc[0], acc[4]), b1 = swap32_add(acc[1], acc[5]);
        const float b2 = swap32_add(acc[2], acc[6]), b3 = swap32_add(acc[3], acc[7]);
        v2f c; c[0] = swap16_add(b0, b2); c[1] = swap16_add(b1, b3);
        *(v2f*)&lds[w * 128 + 8 * n + 2 * q] = c;
    }
    const unsigned msk = ((n & 3) < 2) ? ((n & 1) ? 0xffff0000u : 0x0000ffffu) : 0u;
    unsigned mk[4];
#pragma unroll
    for (int r = 0; r < 4; ++r) mk[r] = ((n >> 2) == r) ? msk : 0u;
    int off1 = MB1_OFF + (8 * cb + w) * BOX1 + ((l >> 2) & 3) * 256 + (4 * q + (l & 3)) * 16;
    asm volatile("" : "+v"(mk[0]), "+v"(mk[1]), "+v"(mk[2]), "+v"(mk[3]), "+v"(off1));
    __syncthreads();
    if (w < 2) {
        const float g = (lds[tid] + lds[128 + tid]) + (lds[256 + tid] + lds[384 + tid]);
        const float u = (lds[512 + tid] + lds[640 + tid]) + (lds[768 + tid] + lds[896 + tid]);
        const v4u p = {f2u(g), tag, f2u(u), tag};
        asm volatile("s_waitcnt vmcnt(4)" ::: "memory");
        __builtin_amdgcn_raw_buffer_store_b128(p, rs_ws, pub1, 0, AUX_SC1);
    }
    __builtin_amdgcn_s_sleep(H1_WAIT);
    float p0, p1, p2, p3;
    poll2(rs_ws, off1, tag, p0, p1, p2, p3);
    unsigned hpk;
    {
        float g = p0 + p2, u = p1 + p3;
        g = dpp_add<DPP_ROR(4)>(g); u = dpp_add<DPP_ROR(4)>(u);
        g = dpp_add<DPP_ROR(8)>(g); u = dpp_add<DPP_ROR(8)>(u);
        g = rbf(g); u = rbf(u);
        const float t = g * fmaf(g * g, -0.10294323970f, -2.30220819814f);
        const float ge = rbf(g * __builtin_amdgcn_rcpf(1.0f + __builtin_amdgcn_exp2f(t)));
        const unsigned hb = f2bf(ge * u);
        hpk = hb | (hb << 16);
    }
    v4f o = {0.f, 0.f, 0.f, 0.f};
#define STEP(i) { const unsigned hh = (unsigned)__builtin_amdgcn_update_dpp(0, (int)hpk, DPP_QUAD(i), 0xf, 0xf, true); \
        const v4u a = {hh & mk[0], hh & mk[1], hh & mk[2], hh & mk[3]}; \
        o = __builtin_amdgcn_mfma_f32_16x16x32_bf16(__builtin_bit_cast(v8s, a), __builtin_bit_cast(v8s, dv[i]), o, 0, 0, 0); }
    STEP(0) STEP(1) STEP(2) STEP(3)
#undef STEP
    {
        v2f c; c[0] = o[0]; c[1] = o[1];
        *(v2f*)&lds[1024 + w * 128 + 8 * n + 2 * q] = c;
    }
    __syncthreads();
    if (w != 0) return;
    {
        v2f s[8];
#pragma unroll
        for (int k = 0; k < 8; ++k) s[k] = *(const v2f*)&lds[1024 + k * 128 + 2 * l];
        const v2f t = ((s[0] + s[1]) + (s[2] + s[3])) + ((s[4] + s[5]) + (s[6] + s[7]));
        const v4u p = {f2u(t[0]), tag, f2u(t[1]), tag};
        __builtin_amdgcn_raw_buffer_store_b128(p, rs_ws, pub2, 0, AUX_SC1);
    }
    if (b >= NRED) return;

    {
        const int o0 = MB2_OFF + b * BOX2 + l * 16;
        float v[8];
        poll4(rs_ws, o0, l < 32 ? o0 + 3072 : o0, tag, v);
        const float s0 = ((v[0] + v[2]) + v[4]) + (l < 32 ? v[6] : 0.f), s1 = ((v[1] + v[3]) + v[5]) + (l < 32 ? v[7] : 0.f);
        float s = swap32_add(s0, s1);
        s = swap16_add(s, s);
        s = dpp_add<DPP_ROR(8)>(s);
        if ((l & 24) == 0) __builtin_amdgcn_raw_buffer_store_b16((unsigned short)f2bf(s), rs_o, (16 * b + 2 * (l & 7) + (l >> 5)) * 2, 0, 0);
        if (b == 0 && l == 0) __hip_atomic_store((gu32*)ws, tag, RLX_AGENT);
    }
}

extern "C" void kernel_launch(void* const* d_in, const int* in_sizes, int n_in,
                              void* d_out, int out_size, void* d_ws, size_t ws_size,
                              hipStream_t stream) {
    standalone__glu_gated_geglu__decode_kernel<<<NWG, 512, 0, stream>>>((const unsigned short*)d_in[0], (const unsigned short*)d_in[1],
        (const unsigned short*)d_in[2], (const unsigned short*)d_in[3], (unsigned short*)d_out, (unsigned char*)d_ws);
}
```
